```python
import torch

def _softmax(x, axis=-1):
    return torch.softmax(x, dim=axis)
def _gelu(x):
    return torch.nn.functional.gelu(x, approximate="tanh")
def _silu(x):
    return torch.nn.functional.silu(x)
def _sigmoid(x):
    return torch.sigmoid(x)
def _layernorm(x, axis=-1, eps=1e-5):
    m = x.mean(dim=axis, keepdim=True)
    v = x.var(dim=axis, keepdim=True, unbiased=False)
    return (x - m) / torch.sqrt(v + eps)
def _scatter_add(tgt, idx, src, axis=0):
    return tgt.clone().index_add_(axis, idx, src)
def _segment_sum(src, seg, n):
    out = torch.zeros((n,) + src.shape[1:], dtype=src.dtype)
    return out.index_add_(0, seg, src)
def _pad(x, pw):
    flat = tuple(v for pair in reversed(pw) for v in pair)
    return torch.nn.functional.pad(x, flat)

kernel_name = 'gated_mix__ssm_conv_s4__dense_activation_silu__prompt'

def setup_inputs():

    _w = (lambda a: (lambda t: t.to(torch.bfloat16) if t.is_floating_point() else t)(torch.as_tensor(a)))
    def _A_setup():
        B, L, D, N = 1, 2048, 512, 128
        A_re = -(lambda _x: torch.exp(torch.as_tensor(_x)))((lambda *s: torch.randn(*s, dtype=torch.bfloat16) * 6.4)(D, N))
        A_im = torch.pi * (lambda *s: torch.randn(*s, dtype=torch.bfloat16) * 0.05)(D, N)
        B_re = (lambda *s: torch.randn(*s, dtype=torch.bfloat16) * 0.2)(D, N)
        B_im = (lambda *s: torch.randn(*s, dtype=torch.bfloat16) * 0.2)(D, N)
        C_re = (lambda *s: torch.randn(*s, dtype=torch.bfloat16) * 0.2)(D, N)
        C_im = (lambda *s: torch.randn(*s, dtype=torch.bfloat16) * 0.2)(D, N)
        log_dt = (lambda *s: torch.randn(*s, dtype=torch.bfloat16) * 0.05)(D) * 0.5 - 4.0
        Dskip = (lambda *s: torch.randn(*s, dtype=torch.bfloat16) * 0.05)(D)
        return {
            "u": _w((lambda *s: torch.randn(*s, dtype=torch.bfloat16) * 0.05)(B, L, D)),
            "A_re": _w(A_re),
            "A_im": _w(A_im),
            "B_re": _w(B_re),
            "B_im": _w(B_im),
            "C_re": _w(C_re),
            "C_im": _w(C_im),
            "log_dt": _w(log_dt),
            "Dskip": _w(Dskip),
        }
    _A_in = _A_setup()

    def _B_setup():
        B, S, D, F = 1, 2048, 512, 1792
        return {
            "x": _w((lambda *s: torch.randn(*s, dtype=torch.bfloat16) * 0.05)(B, S, D)),
            "w1": _w((lambda *s: torch.randn(*s, dtype=torch.bfloat16) * 6.4)(D, F) * (D ** -0.5)),
            "w2": _w((lambda *s: torch.randn(*s, dtype=torch.bfloat16) * 0.2)(F, D) * (F ** -0.5)),
        }
    _B_in = _B_setup()

    return {
        "A_u": _A_in["u"],
        "A_A_re": _A_in["A_re"],
        "A_A_im": _A_in["A_im"],
        "A_B_re": _A_in["B_re"],
        "A_B_im": _A_in["B_im"],
        "A_C_re": _A_in["C_re"],
        "A_C_im": _A_in["C_im"],
        "A_log_dt": _A_in["log_dt"],
        "A_Dskip": _A_in["Dskip"],
        "B_x": _B_in["x"],
        "B_w1": _B_in["w1"],
        "B_w2": _B_in["w2"],
        "gate_w": _w((lambda *s: torch.randn(*s, dtype=torch.bfloat16) * 0.05)(512) * 16.0),
    }

def reference(A_u, A_A_re, A_A_im, A_B_re, A_B_im, A_C_re, A_C_im, A_log_dt, A_Dskip, B_x, B_w1, B_w2, gate_w):
    def _A_ref(u, A_re, A_im, B_re, B_im, C_re, C_im, log_dt, Dskip):
        Bsz, L, D = u.shape
        dt = torch.exp(log_dt)[:, None]
        A = A_re + 1j * A_im
        Bc = B_re + 1j * B_im
        Cc = C_re + 1j * C_im
        logdA = dt * A
        dB = dt * Bc
        l = (lambda x, dt: x.to(dt))(torch.arange(L), A_re.dtype)
        P = torch.exp(logdA[:, :, None] * l[None, None, :])
        K = torch.einsum("dn,dnl,dn->dl", Cc, P, dB).real
        L2 = 2 * L
        Kp = _pad(K, ((0, 0), (0, L)))
        up = (lambda x, *d: x.permute(*d))(u, 0, 2, 1)
        up = _pad(up, ((0, 0), (0, 0), (0, L)))
        Kf = (lambda x, axes=(-2,-1): torch.fft.rfft2(x.float(), dim=axes))(Kp[:, None, :], axes=(-2, -1))
        Uf = (lambda x, axes=(-2,-1): torch.fft.rfft2(x.float(), dim=axes))(up[:, :, None, :], axes=(-2, -1))
        Yf = Uf * Kf[None, :, :, :]
        y = (lambda x, s, axes=(-2,-1): torch.fft.irfft2(x, s=s, dim=axes).to(torch.bfloat16))(Yf, (1, L2), axes=(-2, -1))[:, :, 0, :L]
        y = (lambda x, *d: x.permute(*d))(y, 0, 2, 1) + u * Dskip[None, None, :]
        return y

    def _B_ref(x, w1, w2):
        h = torch.einsum("bsd,df->bsf", x, w1)
        h = _silu(h)
        out = torch.einsum("bsf,fd->bsd", h, w2)
        return out

    x = A_u
    g = _sigmoid((x * gate_w).sum(-1, keepdims=True))
    a = _A_ref(x, A_A_re, A_A_im, A_B_re, A_B_im, A_C_re, A_C_im, A_log_dt, A_Dskip)
    b = _B_ref(x, B_w1, B_w2)
    return g * a + (1 - g) * b
```

```cpp
#include <hip/hip_runtime.h>
#include <hip/hip_bf16.h>
#include <stdint.h>

typedef unsigned short u16;
typedef __attribute__((ext_vector_type(8))) short bf16x8;
typedef __attribute__((ext_vector_type(4))) float f32x4;

constexpr int L = 2048, D = 512, N = 128, F = 1792;

__device__ __forceinline__ float bf2f(u16 v) { return __uint_as_float(((unsigned)v) << 16); }
__device__ __forceinline__ u16 f2bf(float f) {
  unsigned u = __float_as_uint(f);
  u += 0x7fffu + ((u >> 16) & 1u);
  return (u16)(u >> 16);
}
__device__ __forceinline__ float rbf(float f) { return bf2f(f2bf(f)); }

__global__ void kgen(const u16* Are, const u16* Aim, const u16* Bre, const u16* Bim, const u16* Cre, const u16* Cim,
                     const u16* logdt, float* K) {
  __shared__ float sa[N], sb[N], scr[N], sci[N];
  int d = blockIdx.x, t = threadIdx.x;
  float dt = rbf(expf(bf2f(logdt[d])));
  if (t < N) {
    float ar = dt * bf2f(Are[d * N + t]), ai = dt * bf2f(Aim[d * N + t]);
    float br = dt * bf2f(Bre[d * N + t]), bi = dt * bf2f(Bim[d * N + t]);
    float cr = bf2f(Cre[d * N + t]), ci = bf2f(Cim[d * N + t]);
    sa[t] = ar; sb[t] = ai;
    scr[t] = cr * br - ci * bi; sci[t] = cr * bi + ci * br;
  }
  __syncthreads();
  for (int l = t; l < L; l += blockDim.x) {
    float lv = rbf((float)l);
    float acc = 0.f;
    for (int n = 0; n < N; ++n) {
      float e = expf(sa[n] * lv);
      float s, c;
      sincosf(sb[n] * lv, &s, &c);
      acc += e * (scr[n] * c - sci[n] * s);
    }
    K[d * L + l] = acc;
  }
}

__global__ void convk(const u16* u, const float* K, const u16* Dskip, u16* a) {
  __shared__ float sK[L], su[L];
  int d = blockIdx.x, t = threadIdx.x;
  for (int l = t; l < L; l += blockDim.x) { sK[l] = K[d * L + l]; su[l] = bf2f(u[l * D + d]); }
  __syncthreads();
  float ds = bf2f(Dskip[d]);
  for (int l = t; l < L; l += blockDim.x) {
    float acc = 0.f;
    for (int m = 0; m <= l; ++m) acc += sK[l - m] * su[m];
    float y = rbf(acc);
    a[l * D + d] = f2bf(y + rbf(su[l] * ds));
  }
}

template <int MODE>
__global__ void gemm(const u16* A, const u16* B, u16* C, int M, int Nn, int Kk, const u16* x, const u16* gw, const u16* a) {
  __shared__ u16 sA[64][40], sB[64][40];
  __shared__ float sg[64];
  int t = threadIdx.x, lane = t & 63, w = t >> 6;
  int bm = blockIdx.y * 64, bn = blockIdx.x * 64;
  f32x4 acc[2][2] = {};
  int wm = (w >> 1) * 32, wn = (w & 1) * 32;
  for (int k0 = 0; k0 < Kk; k0 += 32) {
    for (int i = t; i < 64 * 32; i += 256) {
      int r = i >> 5, c = i & 31;
      sA[r][c] = A[(bm + r) * Kk + k0 + c];
    }
    for (int i = t; i < 64 * 32; i += 256) {
      int k = i >> 6, n = i & 63;
      sB[n][k] = B[(k0 + k) * Nn + bn + n];
    }
    __syncthreads();
    for (int i = 0; i < 2; ++i)
      for (int j = 0; j < 2; ++j) {
        bf16x8 fa = *(const bf16x8*)&sA[wm + i * 16 + (lane & 15)][(lane >> 4) * 8];
        bf16x8 fb = *(const bf16x8*)&sB[wn + j * 16 + (lane & 15)][(lane >> 4) * 8];
        acc[i][j] = __builtin_amdgcn_mfma_f32_16x16x32_bf16(fa, fb, acc[i][j], 0, 0, 0);
      }
    __syncthreads();
  }
  if (MODE == 1) {
    if (t < 64) {
      float s = 0.f;
      for (int k = 0; k < D; ++k) s += rbf(bf2f(x[(bm + t) * D + k]) * bf2f(gw[k]));
      s = rbf(s);
      sg[t] = rbf(1.f / (1.f + expf(-s)));
    }
    __syncthreads();
  }
  for (int i = 0; i < 2; ++i)
    for (int j = 0; j < 2; ++j)
      for (int r = 0; r < 4; ++r) {
        int row = wm + i * 16 + (lane >> 4) * 4 + r, col = bn + wn + j * 16 + (lane & 15);
        float v = rbf(acc[i][j][r]);
        if (MODE == 0) {
          C[(bm + row) * Nn + col] = f2bf(v / (1.f + expf(-v)));
        } else {
          float g = sg[row];
          float av = bf2f(a[(bm + row) * Nn + col]);
          C[(bm + row) * Nn + col] = f2bf(rbf(g * av) + rbf(rbf(1.f - g) * v));
        }
      }
}

extern "C" void kernel_launch(void* const* d_in, const int* in_sizes, int n_in, void* d_out, int out_size, void* d_ws,
                              size_t ws_size, hipStream_t stream) {
  const u16* u = (const u16*)d_in[0];
  const u16 *Are = (const u16*)d_in[1], *Aim = (const u16*)d_in[2], *Bre = (const u16*)d_in[3], *Bim = (const u16*)d_in[4];
  const u16 *Cre = (const u16*)d_in[5], *Cim = (const u16*)d_in[6], *logdt = (const u16*)d_in[7], *Dskip = (const u16*)d_in[8];
  const u16 *w1 = (const u16*)d_in[10], *w2 = (const u16*)d_in[11], *gw = (const u16*)d_in[12];
  char* ws = (char*)d_ws;
  float* K = (float*)ws;
  u16* a = (u16*)(ws + (size_t)D * L * 4);
  u16* h = a + (size_t)L * D;
  kgen<<<D, 256, 0, stream>>>(Are, Aim, Bre, Bim, Cre, Cim, logdt, K);
  convk<<<D, 256, 0, stream>>>(u, K, Dskip, a);
  gemm<0><<<dim3(F / 64, L / 64), 256, 0, stream>>>(u, w1, h, L, F, D, nullptr, nullptr, nullptr);
  gemm<1><<<dim3(D / 64, L / 64), 256, 0, stream>>>(h, w2, (u16*)d_out, L, D, F, u, gw, a);
}
```

```cpp
#include <hip/hip_runtime.h>
#include <stdint.h>
#include <type_traits>

typedef unsigned short u16;
typedef unsigned int u32;
typedef unsigned long long u64;
typedef short s16x4 __attribute__((ext_vector_type(4)));
typedef short s16x8 __attribute__((ext_vector_type(8)));
typedef float f32x2 __attribute__((ext_vector_type(2)));
typedef float f32x4 __attribute__((ext_vector_type(4)));
typedef u32 u32x2 __attribute__((ext_vector_type(2)));
typedef u32 u32x4 __attribute__((ext_vector_type(4)));
typedef __bf16 bf16x2 __attribute__((ext_vector_type(2)));
typedef __attribute__((address_space(3))) char lds_char;
typedef __attribute__((address_space(1))) u64 g_u64;
typedef __amdgpu_buffer_rsrc_t rsrc_t;

#define STAGE 9
#define CPRIO 2
#define PACE 0

constexpr int SL = 2048, SD = 512, SN = 128, SF = 1792;

constexpr u32 WS_EPOCH = 0;
constexpr u32 WS_HFLAG = 256;
constexpr u32 WS_UFLAG = 4096;
constexpr u32 WS_WFLAG = 8192;
constexpr u32 WS_GFLAG = 8704;
constexpr u32 WS_AFLAG = 10240;
constexpr u32 WS_GATE = 16384;
constexpr u32 WS_TS = 32768;
constexpr u32 WS_HF = 1u << 20;
constexpr u32 WS_W2F = WS_HF + 128u * 56u * 1024u;
constexpr u32 WS_UT = WS_W2F + 32u * 56u * 1024u;
constexpr u32 WS_AT = WS_UT + 512u * 4096u;
constexpr u32 WS_USED = WS_AT + 512u * 4096u;

constexpr u32 L_TAB = 98304;
constexpr u32 TAB_ROW = 528, TAB_CH = 44 * TAB_ROW;
constexpr u32 L_UC = 98304;
constexpr u32 UC_B = 4640, UC_CH = 4 * UC_B;
constexpr u32 L_R = 144768;
constexpr u32 R_CH = 4224;
constexpr u32 L_STG = 153216;
constexpr u32 L_MISC = 161408;
constexpr u32 PART_W = 16 * 272, PART_LB = 8 * PART_W;
constexpr u32 L_AT = 139264;
constexpr u32 L_RED = 73728;
constexpr u32 LDS_BYTES = 163840;

constexpr int AUX_SC1 = 16;
constexpr u32 SPIN_CAP = 1u << 14;
constexpr float LOG2E = 1.4426950408889634f;

__device__ __forceinline__ float bf2f(u32 v) { return __uint_as_float(v << 16); }
__device__ __forceinline__ u32 pk2(float lo, float hi) { bf16x2 p = {(__bf16)lo, (__bf16)hi}; return __builtin_bit_cast(u32, p); }
__device__ __forceinline__ float rbf(float v) { return __uint_as_float(pk2(v, v) << 16); }
__device__ __forceinline__ rsrc_t mk(const void* p, u32 n) { return __builtin_amdgcn_make_buffer_rsrc((void*)p, (short)0, (int)n, 0x00020000); }
__device__ __forceinline__ void bar() { asm volatile("s_waitcnt lgkmcnt(0)\n\ts_barrier" ::: "memory"); }
#define WAITV(n) asm volatile("s_waitcnt vmcnt(" #n ")" ::: "memory")
#define SB() __builtin_amdgcn_sched_barrier(0)
__device__ __forceinline__ void glds(rsrc_t rs, u32 dst, u32 voff, u32 soff) {
  asm volatile("s_mov_b32 m0, %0\n\ts_nop 0\n\tbuffer_load_dwordx4 %1, %2, %3 offen lds" :: "s"(dst), "v"(voff), "s"(rs), "s"(soff) : "memory");
}
__device__ __forceinline__ void glds2(rsrc_t rs, u32 dbase, int dimm, u32 voff, u32 sbase, int simm) {
  u32 so;
  asm volatile("s_add_u32 m0, %1, %2\n\ts_add_u32 %0, %5, %6\n\tbuffer_load_dwordx4 %3, %4, %0 offen lds"
               : "=&s"(so) : "s"(dbase), "i"(dimm), "v"(voff), "s"(rs), "s"(sbase), "i"(simm) : "memory", "scc");
}
__device__ __forceinline__ u32x4 ald128(rsrc_t rs, u32 voff) { u32x4 v; asm volatile("buffer_load_dwordx4 %0, %1, %2, 0 offen sc1" : "=v"(v) : "v"(voff), "s"(rs) : "memory"); return v; }
__device__ __forceinline__ u32x2 ald64(rsrc_t rs, u32 voff) { u32x2 v; asm volatile("buffer_load_dwordx2 %0, %1, %2, 0 offen sc1" : "=v"(v) : "v"(voff), "s"(rs) : "memory"); return v; }
#define READY(v) asm volatile("" : "+v"(v))
template <class T> __device__ __forceinline__ T ldsr(lds_char* b, u32 off) { return *(const __attribute__((address_space(3))) T*)(b + off); }
template <class T> __device__ __forceinline__ void ldsw(lds_char* b, u32 off, T v) { *(__attribute__((address_space(3))) T*)(b + off) = v; }
__device__ __forceinline__ s16x4 ldstr(lds_char* b, u32 off) {
  return __builtin_amdgcn_ds_read_tr16_b64_v4i16((__attribute__((address_space(3))) s16x4*)(b + off));
}
__device__ __forceinline__ s16x8 cat(s16x4 a, s16x4 b) { return __builtin_shufflevector(a, b, 0, 1, 2, 3, 4, 5, 6, 7); }
__device__ __forceinline__ f32x4 mfma(s16x8 a, s16x8 b, f32x4 c) { return __builtin_amdgcn_mfma_f32_16x16x32_bf16(a, b, c, 0, 0, 0); }
__device__ __forceinline__ s16x8 ld_sc1(rsrc_t rs, u32 voff, u32 soff) {
  return __builtin_bit_cast(s16x8, __builtin_amdgcn_raw_buffer_load_b128(rs, voff, soff, AUX_SC1));
}

__device__ __forceinline__ void flag_store(char* ws, u32 off, u64 tag) {
  __hip_atomic_store((g_u64*)(ws + off), tag, __ATOMIC_RELAXED, __HIP_MEMORY_SCOPE_AGENT);
}
__device__ __forceinline__ void poll_flags(char* ws, u32 off, int n, u64 tag, int lane) {
  g_u64* p = (g_u64*)(ws + off) + (lane < n ? lane : 0);
#pragma unroll 1
  for (u32 spin = 0; spin < SPIN_CAP; ++spin) {
    u64 v = __hip_atomic_load(p, __ATOMIC_RELAXED, __HIP_MEMORY_SCOPE_AGENT);
    if (__all(v == tag)) break;
    __builtin_amdgcn_s_sleep(1);
  }
  asm volatile("" ::: "memory");
}
__device__ __forceinline__ bool last_arrival(lds_char* lds, u32 off, u32 n, int lane) {
  u32 old = 0;
  if (lane == 0) old = __hip_atomic_fetch_add((__attribute__((address_space(3))) u32*)(lds + off), 1u, __ATOMIC_RELAXED, __HIP_MEMORY_SCOPE_WORKGROUP);
  old = __builtin_amdgcn_readfirstlane(old);
  return old == n - 1;
}
__device__ __forceinline__ u64 read_tag(lds_char* lds) {
  u32x2 t = ldsr<u32x2>(lds, L_MISC);
  return ((u64)(u32)__builtin_amdgcn_readfirstlane(t.y) << 32) | (u32)__builtin_amdgcn_readfirstlane(t.x);
}

#define STAMP(i) do {} while (0)
#define STAMP_DECL
#define STAMP_FLUSH() do {} while (0)

struct cf { float re, im; };
__device__ __forceinline__ cf cmul(cf a, cf b) {
  float t1, t2; cf r;
  asm("v_mul_f32 %0, %1, %2" : "=v"(t1) : "v"(a.im), "v"(b.im));
  asm("v_mul_f32 %0, %1, %2" : "=v"(t2) : "v"(a.im), "v"(b.re));
  asm("v_fma_f32 %0, %1, %2, -%3" : "=v"(r.re) : "v"(a.re), "v"(b.re), "v"(t1));
  asm("v_fma_f32 %0, %1, %2, %3" : "=v"(r.im) : "v"(a.re), "v"(b.im), "v"(t2));
  return r;
}
__device__ __forceinline__ cf zpow(float l2, float rev, float m) {
  float e = __builtin_amdgcn_exp2f(l2 * m);
  float a = __builtin_amdgcn_fractf(rev * m);
  cf r; r.re = e * __builtin_amdgcn_cosf(a); r.im = e * __builtin_amdgcn_sinf(a);
  return r;
}

template <int SEG> __device__ __forceinline__ void kgen_job(lds_char* lds, int c, int lane) {
  const int j = lane & 15, kq = lane >> 4;
  const int rowA = SEG == 0 ? 16 + j : (SEG == 1 ? 32 : (SEG == 2 ? 16 : 28)) + 3 * (j >> 2) + ((j & 3) < 2 ? (j & 3) : 2);
  const u32 pa = L_TAB + c * TAB_CH + rowA * TAB_ROW + kq * 16, pb = L_TAB + c * TAB_CH + j * TAB_ROW + kq * 16;
  f32x4 acc = {0, 0, 0, 0};
#pragma unroll
  for (int s = 0; s < 8; ++s) acc = mfma(ldsr<s16x8>(lds, pa + s * 64), ldsr<s16x8>(lds, pb + s * 64), acc);
  const u32 rb = L_R + c * R_CH;
  const int i = j, q = kq;
  if (SEG == 0) {
    ldsw<u32x2>(lds, rb + 2 * (2044 - 16 * i - 4 * q), u32x2{pk2(acc[3], acc[2]), pk2(acc[1], acc[0])});
  } else if (SEG == 1) {
    ldsw<u32x2>(lds, rb + 2 * (2044 - 256 - 16 * i - 4 * q), u32x2{pk2(acc[2], acc[1]), pk2(acc[0], acc[0])});
  } else if (SEG == 2) {
    const int lag0 = 512 + 256 * (q >> 1) + 16 * i + 8 * (q & 1);
    ldsw<u32x4>(lds, rb + 2 * (2040 - lag0), u32x4{pk2(acc[2], acc[2]), pk2(acc[1], acc[1]), pk2(acc[1], acc[0]), pk2(acc[0], acc[0])});
  } else {
    const int lag0 = 1024 + 256 * q + 16 * i;
    const u32 x00 = pk2(acc[0], acc[0]), x11 = pk2(acc[1], acc[1]), x22 = pk2(acc[2], acc[2]), x10 = pk2(acc[1], acc[0]);
    ldsw<u32x4>(lds, rb + 2 * (2032 - lag0), u32x4{x22, x22, x11, x11});
    ldsw<u32x4>(lds, rb + 2 * (2040 - lag0), u32x4{x11, x10, x00, x00});
  }
}

constexpr int CONV_LEAD = 10;
template <int H> __device__ __forceinline__ void conv_wave(lds_char* lds, int c, int lane, f32x4 (&acc)[8]) {
  const int r = lane & 15, kq = lane >> 4;
  u32 ub = L_UC + c * UC_CH + (lane & 3) * UC_B + 2 * (192 + 64 * ((lane >> 2) & 3) + 32 + 8 * kq) - 64 * (4 * H + 3);
  u32 kb0 = L_R + c * R_CH + 2 * (2044 - 4 * r + 32 + 8 * kq) - 64 * (4 * H + 3) - 512 * 7, kb1 = kb0 + 2048;
  asm("" : "+v"(ub)); asm("" : "+v"(kb0)); asm("" : "+v"(kb1));
  s16x8 kf[4][8], uf[4][8];
#pragma unroll
  for (int i = 0; i < 8; ++i) acc[i] = f32x4{0, 0, 0, 0};
#pragma unroll
  for (int sl = 0; sl < 4; ++sl)
#pragma unroll
    for (int j = 0; j < 8; ++j)
#pragma unroll
      for (int d = 0; d <= j; ++d) {
        const int g = 36 * sl + j * (j + 1) / 2 + d;
#pragma unroll
        for (int s2 = 0; s2 < 4; ++s2)
#pragma unroll
          for (int j2 = 0; j2 < 8; ++j2) {
            const int first = 36 * s2 + j2 * (j2 + 1) / 2 - CONV_LEAD;
            if ((first < 0 ? 0 : first) == g) {
              const u32 ko = (j2 < 4 ? kb1 : kb0) + 64 * (3 - s2) + 512 * ((7 - j2) & 3);
              kf[s2][j2] = cat(ldsr<s16x4>(lds, ko), ldsr<s16x4>(lds, ko + 8));
              uf[s2][7 - j2] = ldsr<s16x8>(lds, ub + 64 * (3 - s2) + 512 * (7 - j2));
            }
          }
        acc[7 - j + d] = mfma(uf[sl][7 - j], kf[sl][d], acc[7 - j + d]);
        SB();
      }
}

__global__ __launch_bounds__(512, 2) void fused(const u16* __restrict__ x, const u16* __restrict__ Are, const u16* __restrict__ Aim,
    const u16* __restrict__ Bre, const u16* __restrict__ Bim, const u16* __restrict__ Cre, const u16* __restrict__ Cim,
    const u16* __restrict__ logdt, const u16* __restrict__ Dskip, const u16* __restrict__ w1, const u16* __restrict__ w2,
    const u16* __restrict__ gw, u16* __restrict__ out, char* __restrict__ ws) {
  extern __shared__ __attribute__((aligned(16))) char smem[];
  lds_char* lds = (lds_char*)smem;
  const int tid = threadIdx.x, lane = tid & 63;
  const int w = __builtin_amdgcn_readfirstlane(tid >> 6);
  const int b = blockIdx.x, xcd = b & 7, idx = b >> 3;
  const bool spare = idx >= 28;
  const rsrc_t rsx = mk(x, SL * SD * 2), rsw = mk(ws, WS_USED);
  const int panel = 4 * (xcd >> 1) + (spare ? 0 : idx / 7), slice = 7 * (xcd & 1) + (spare ? 0 : idx % 7);
  const int ch0 = 64 * xcd + 2 * idx;
  const int rb = 4 * xcd + (idx >> 3), ct = idx & 7;
  const int sq = 4 * xcd + (idx - 28);
  u64 tag = 0;
  s16x8 wf2[7][4];
  STAMP_DECL;
  STAMP(0);

  if (w < 4) {
    const int g = lane >> 4, q4 = (lane >> 2) & 3, p = lane & 3;
    const u32 v128 = (u32)(lane >> 3) * 1024 + (u32)(((lane & 7) >> 1) ^ ((lane >> 4) & 3)) * 32 + (lane & 1) * 16;
#pragma unroll
    for (int i = 0; i < 2; ++i) glds(rsx, L_STG + (w * 2 + i) * 1024, v128, (u32)(64 * idx + 16 * w + 8 * i) * 1024 + xcd * 128);
    if (spare) {
      const rsrc_t rs2 = mk(w2, SF * SD * 2);
#pragma unroll
      for (int i = 0; i < 14; ++i) glds(rs2, (w * 14 + i) * 1024, v128, (u32)(448 * (sq & 3) + (w * 14 + i) * 8) * 1024 + (sq >> 2) * 128);
    }
    float sd[6];
    {
      const int d = ch0 + (w >> 1), o = d * SN + (tid & 127);
      const float dt = rbf(__expf(bf2f(logdt[d])));
      const float ar = dt * bf2f(Are[o]), ai = dt * bf2f(Aim[o]), br = dt * bf2f(Bre[o]), bi = dt * bf2f(Bim[o]);
      const float cr = bf2f(Cre[o]), ci = bf2f(Cim[o]);
      const float l2 = ar * LOG2E, rev = ai * 0.15915494309189535f;
      cf z = zpow(l2, rev, 1.f), z256 = zpow(l2, rev, 256.f);
      sd[0] = z.re; sd[1] = z.im; sd[2] = z256.re; sd[3] = z256.im;
      sd[4] = cr * br - ci * bi; sd[5] = cr * bi + ci * br;
    }
    WAITV(0);
    {
#pragma unroll
      for (int m = 0; m < 2; ++m) {
        const int cb = 2 * m + (g >> 1), r0 = 8 * (g & 1) + q4, r1 = r0 + 4;
        s16x4 lo = ldstr(lds, L_STG + w * 2048 + r0 * 128 + ((cb ^ ((r0 >> 1) & 3)) * 32) + p * 8);
        s16x4 hi = ldstr(lds, L_STG + w * 2048 + r1 * 128 + ((cb ^ ((r1 >> 1) & 3)) * 32) + p * 8);
        u32 off = WS_UT + (u32)(64 * xcd + 16 * cb + (lane & 15)) * 4096 + (u32)(64 * idx + 16 * w + 8 * (g & 1)) * 2;
        __builtin_amdgcn_raw_buffer_store_b128(__builtin_bit_cast(u32x4, cat(lo, hi)), rsw, off, 0, AUX_SC1);
      }
      asm volatile("s_waitcnt lgkmcnt(0)" ::: "memory");
#pragma unroll
      for (int k = 0; k < 6; ++k) ldsw<float>(lds, L_STG + w * 2048 + k * 256 + lane * 4, sd[k]);
    }
    __builtin_amdgcn_s_setprio(CPRIO);
    STAMP(1);
    bar();
    tag = read_tag(lds);
    STAMP(2);
    if (!spare) {
      const int wf = w & 1, wl = w >> 1;
      const int r = lane & 15;
      const u32 xb = wl * 8192 + r * 128 + (((g ^ (r >> 1)) & 7) << 4);
      const int swz = q4 | ((g & 1) << 2);
      u32 wb[4];
#pragma unroll
      for (int ft = 0; ft < 4; ++ft) wb[ft] = 16384 + (8 * g + q4) * 256 + (((4 * wf + ft) ^ swz) << 5) + p * 8;
      f32x4 acc[4][4];
#pragma unroll
      for (int i = 0; i < 4; ++i)
#pragma unroll
        for (int j = 0; j < 4; ++j) acc[i][j] = f32x4{0, 0, 0, 0};
      s16x8 fa[2][4], fb[2][4];
      auto rdA = [&](int j, int ft) { u32 o = ((j >> 1) % 3) * 32768 + (j & 1) * 8192; return cat(ldstr(lds, wb[ft] + o), ldstr(lds, wb[ft] + o + 1024)); };
      auto rdB = [&](int j, int lt) { u32 o = ((j >> 1) % 3) * 32768 + lt * 2048; return ldsr<s16x8>(lds, (xb ^ ((j & 1) << 6)) + o); };
#pragma unroll
      for (int i = 0; i < 4; ++i) { fa[0][i] = rdA(0, i); fb[0][i] = rdB(0, i); }
#pragma unroll
      for (int j = 0; j < 16; ++j) {
        const int c = j & 1, n = c ^ 1;
        if (j == 3) WAITV(0);
        if (c == 1 && j < 15) { STAMP(8 + (j >> 1)); bar(); }
        if (j == 3 && w == 0 && lane == 0) flag_store(ws, WS_UFLAG + (xcd * 32 + idx) * 8, tag);
        SB();
#pragma unroll
        for (int m = 0; m < 16; ++m) {
          const int ft = m >> 2, lt = m & 3;
          acc[ft][lt] = mfma(fa[c][ft], fb[c][lt], acc[ft][lt]);
          if (j < 15) {
            if (m < 4) fb[n][m] = rdB(j + 1, m);
            else if (m < 8) fa[n][m - 4] = rdA(j + 1, m - 4);
          }
          SB();
        }
      }
      STAMP(3);
      bar();
      STAMP(4);
#pragma unroll
      for (int m = 0; m < 2; ++m)
#pragma unroll
        for (int lt = 0; lt < 4; ++lt) {
          f32x2 v[4], t[4];
          v[0] = acc[2 * m][lt].xy; v[1] = acc[2 * m][lt].zw; v[2] = acc[2 * m + 1][lt].xy; v[3] = acc[2 * m + 1][lt].zw;
#pragma unroll
          for (int i = 0; i < 4; ++i) t[i] = v[i] * -LOG2E;
          SB();
#pragma unroll
          for (int i = 0; i < 4; ++i) { t[i].x = __builtin_amdgcn_exp2f(t[i].x); t[i].y = __builtin_amdgcn_exp2f(t[i].y); }
          SB();
#pragma unroll
          for (int i = 0; i < 4; ++i) t[i] = t[i] + 1.f;
          SB();
#pragma unroll
          for (int i = 0; i < 4; ++i) { t[i].x = __builtin_amdgcn_rcpf(t[i].x); t[i].y = __builtin_amdgcn_rcpf(t[i].y); }
          SB();
          u32x4 o;
#pragma unroll
          for (int i = 0; i < 4; ++i) { f32x2 s = v[i] * t[i]; o[i] = pk2(s.x, s.y); }
          u32 off = WS_HF + (u32)((panel * 8 + 4 * wl + lt) * 56 + slice * 4 + 2 * wf + m) * 1024;
          __builtin_amdgcn_raw_buffer_store_b128(o, rsw, lane * 16 + off, 0, AUX_SC1);
          SB();
        }
      STAMP(5);
      bar();
      __builtin_amdgcn_s_setprio(0);
      WAITV(0);
      STAMP(6);
      if (last_arrival(lds, L_MISC + 16, 4, lane) && lane == 0) flag_store(ws, WS_HFLAG + (panel * 16 + slice) * 8, tag);
    } else {
#pragma unroll
      for (int si = 0; si < 14; ++si) {
        if (si == 5) bar();
        if (si == 10) {
          WAITV(10); bar();
          if (w == 0 && lane == 0) flag_store(ws, WS_UFLAG + (xcd * 32 + idx) * 8, tag);
        }
        const int r0 = 32 * si + 4 * g + q4, r1 = r0 + 16;
        s16x4 lo = ldstr(lds, r0 * 128 + ((w ^ ((r0 >> 1) & 3)) * 32) + p * 8);
        s16x4 hi = ldstr(lds, r1 * 128 + ((w ^ ((r1 >> 1) & 3)) * 32) + p * 8);
        u32 off = WS_W2F + (u32)((4 * (sq >> 2) + w) * 56 + 14 * (sq & 3) + si) * 1024;
        __builtin_amdgcn_raw_buffer_store_b128(__builtin_bit_cast(u32x4, cat(lo, hi)), rsw, lane * 16 + off, 0, AUX_SC1);
      }
      bar();
      WAITV(0);
      bar();
      if (w == 0 && lane == 0) flag_store(ws, WS_WFLAG + sq * 8, tag);
#pragma unroll
      for (int i = 5; i <= 8; ++i) bar();
      __builtin_amdgcn_s_setprio(0);
      STAMP(4);
      {
        const u32x4 gv = *(const u32x4*)(gw + lane * 8);
        float keep = 0.f;
#pragma unroll
        for (int h = 0; h < 2; ++h) {
          u32x4 xr[8];
#pragma unroll
          for (int i = 0; i < 8; ++i) xr[i] = *(const u32x4*)(x + (size_t)(64 * sq + 16 * w + 8 * h + i) * SD + lane * 8);
#pragma unroll
          for (int i = 0; i < 8; ++i) {
            float s = 0.f;
#pragma unroll
            for (int k = 0; k < 4; ++k) {
              u32 pr = pk2(bf2f(xr[i][k] & 0xffffu) * bf2f(gv[k] & 0xffffu), __uint_as_float(xr[i][k] & 0xffff0000u) * __uint_as_float(gv[k] & 0xffff0000u));
              s += __uint_as_float(pr << 16) + __uint_as_float(pr & 0xffff0000u);
            }
#pragma unroll
            for (int o = 32; o >= 1; o >>= 1) s += __shfl_xor(s, o);
            s = rbf(s);
            s = rbf(1.f / (1.f + __expf(-s)));
            if (lane == 8 * h + i) keep = s;
          }
        }
        if (lane < 16) __builtin_amdgcn_raw_buffer_store_b32(__float_as_uint(keep), rsw, (u32)(64 * sq + 16 * w + lane) * 4, WS_GATE, AUX_SC1);
      }
      STAMP(5);
      bar();
      WAITV(0);
      if (last_arrival(lds, L_MISC + 16, 4, lane) && lane == 0) flag_store(ws, WS_GFLAG + sq * 8, tag);
      if (sq == 0 && w == 0) {
#pragma unroll
        for (int i = 0; i < 4; ++i) poll_flags(ws, WS_UFLAG + i * 512, 64, tag, lane);
        if (lane == 0) flag_store(ws, WS_EPOCH, tag);
      }
    }
#pragma unroll
    for (int i = 0; i < 7; ++i)
#pragma unroll
      for (int db = 0; db < 4; ++db) wf2[i][db] = ld_sc1(rsw, lane * 16, WS_W2F + (u32)((4 * ct + db) * 56 + 7 * w + i) * 1024);
    poll_flags(ws, WS_HFLAG + (rb >> 1) * 128, 14, tag, lane);
    STAMP(7);
  } else {
    const int aw = w - 4, t = tid - 256;
    const int c = aw >> 1, n = t & 127;
    const rsrc_t rs1 = mk(w1, SD * SF * 2);
    const u32 vx = (u32)(lane >> 3) * 1024 + (u32)((lane & 7) ^ (lane >> 4)) * 16;
    const u32 cw = (u32)((((lane & 15) >> 1) ^ (lane >> 4)) << 5) | ((lane & 1) << 4);
    const u32 vx0 = vx, vx1 = vx ^ 64, vw0 = (u32)(lane >> 4) * 3584 + cw, vw1 = (u32)(lane >> 4) * 3584 + (cw ^ 128);
    const u32 sA = (u32)aw * 4096, sX = (u32)panel * 131072 + (u32)aw * 32768, sW = (u32)aw * (16 * 3584) + (u32)slice * 256;
    float dsk;
    auto run = [&](auto SPARE) {
      constexpr bool sp = decltype(SPARE)::value;
      auto ld = [&](int s, int k) {
        if (sp) return;
        const int i = k >> 1, d = (s % 3) * 32768;
        if (k & 1) glds2(rs1, sA, d + 16384 + i * 1024, (i & 2) ? vw1 : vw0, sW, (64 * s + 4 * i) * 3584);
        else glds2(rsx, sA, d + i * 1024, (i & 1) ? vx1 : vx0, sX, i * 8192 + s * 128);
      };
      u32x2 epv = ald64(rsw, WS_EPOCH);
      const u32 dskb = Dskip[ch0 + c];
#pragma unroll
      for (int k = 0; k < 16; ++k) ld(k >> 3, k & 7);
      if (sp) WAITV(0); else WAITV(16);
      READY(epv);
      dsk = bf2f(dskb);
      if (aw == 0 && lane == 0) {
        const u64 ep = (((u64)epv.y << 32) | epv.x) + 1;
        ldsw<u32x2>(lds, L_MISC, u32x2{(u32)ep, (u32)(ep >> 32)});
        ldsw<u32x4>(lds, L_MISC + 16, u32x4{0, 0, 0, 0});
        ldsw<u32x4>(lds, L_MISC + 32, u32x4{0, 0, 0, 0});
      }
      WAITV(8);
      STAMP(1);
      bar();
      tag = read_tag(lds);
      STAMP(2);
      const u32 tb = L_TAB + c * TAB_CH + n * 4;
      auto put = [&](int row, cf v) { ldsw<u32>(lds, tb + row * TAB_ROW, pk2(v.re, v.im)); };
      cf z, z256, cc;
      z.re = ldsr<float>(lds, L_STG + aw * 2048 + 0 * 256 + lane * 4); z.im = ldsr<float>(lds, L_STG + aw * 2048 + 1 * 256 + lane * 4);
      z256.re = ldsr<float>(lds, L_STG + aw * 2048 + 2 * 256 + lane * 4); z256.im = ldsr<float>(lds, L_STG + aw * 2048 + 3 * 256 + lane * 4);
      cc.re = ldsr<float>(lds, L_STG + aw * 2048 + 4 * 256 + lane * 4); cc.im = ldsr<float>(lds, L_STG + aw * 2048 + 5 * 256 + lane * 4);
      cf ev[9];
      {
        ld(2, 0);
        cf pz = z;
        put(16, cf{1.f, 0.f}); put(17, z);
        ld(2, 1);
#pragma unroll
        for (int j = 2; j <= 16; ++j) {
          pz = cmul(pz, z);
          if (j < 16) put(16 + j, pz);
          if ((j & 1) == 0) ev[j >> 1] = pz;
          if ((j & 1) && j < 15) ld(2, (j + 1) >> 1);
        }
      }
      WAITV(8); STAMP(9); bar();
      {
        cf pp = cc;
        ld(3, 0);
        put(0, cf{pp.re, -pp.im});
#pragma unroll
        for (int i = 1; i < 16; ++i) {
          pp = cmul(pp, ev[8]); put(i, cf{pp.re, -pp.im});
          if (i & 1) { if (i < 15) ld(3, (i + 1) >> 1); }
        }
      }
      WAITV(8); STAMP(10); bar();
      cf z512, z768;
      {
        ld(4, 0);
        put(32, z256);
#pragma unroll
        for (int k = 1; k <= 8; ++k) {
          cf v = cmul(z256, ev[k]);
          if (k & 1) put(32 + 3 * (k >> 1) + 1, v);
          else { put(32 + 3 * ((k >> 1) - 1) + 2, v); if (k < 8) put(32 + 3 * (k >> 1), v); }
          if (k < 8) ld(4, k);
        }
        z512 = cmul(z256, z256); z768 = cmul(z512, z256);
      }
      WAITV(8); STAMP(11); bar();
      u32 q2[10];
      {
        ld(5, 0);
        if (aw & 1) kgen_job<1>(lds, c, lane); else kgen_job<0>(lds, c, lane);
#pragma unroll
        for (int a = 0; a < 2; ++a) {
          cf base = a ? z768 : z512;
          q2[5 * a] = pk2(base.re, base.im);
#pragma unroll
          for (int k = 1; k <= 4; ++k) {
            cf v = cmul(base, ev[2 * k]); q2[5 * a + k] = pk2(v.re, v.im);
            SB();
            if (4 * a + k < 8) ld(5, 4 * a + k);
          }
        }
      }
      WAITV(8); STAMP(12); bar();
      {
        ld(6, 0);
#pragma unroll
        for (int a = 0; a < 2; ++a) {
          const u32 o = tb + (16 + 6 * a) * TAB_ROW;
          ldsw<u32>(lds, o, q2[5 * a]); ldsw<u32>(lds, o + TAB_ROW, q2[5 * a + 1]); ldsw<u32>(lds, o + 2 * TAB_ROW, q2[5 * a + 2]);
          ldsw<u32>(lds, o + 3 * TAB_ROW, q2[5 * a + 2]); ldsw<u32>(lds, o + 4 * TAB_ROW, q2[5 * a + 3]); ldsw<u32>(lds, o + 5 * TAB_ROW, q2[5 * a + 4]);
          ld(6, 1 + a);
        }
        cf base = cmul(z512, z512);
#pragma unroll
        for (int q = 0; q < 4; ++q) {
          cf v8 = cmul(base, ev[4]), v16 = cmul(base, ev[8]);
          put(28 + 3 * q, base); put(28 + 3 * q + 1, v8); put(28 + 3 * q + 2, v16);
          if (q < 3) base = cmul(base, z256);
          ld(6, 3 + q);
        }
        ld(6, 7);
        if (t < 64) ldsw<u32>(lds, L_R + (t >> 5) * R_CH + 4096 + (t & 31) * 4, 0u);
      }
      WAITV(8); STAMP(13); bar();
      u32x2 pu = ald64(rsw, WS_UFLAG + (xcd * 32 + (lane & 31)) * 8);
      u32x2 pw = ald64(rsw, WS_WFLAG + (ct * 4 + (lane & 3)) * 8);
#pragma unroll
      for (int k = 0; k < 4; ++k) ld(7, k);
      if (aw & 1) kgen_job<3>(lds, c, lane); else kgen_job<2>(lds, c, lane);
#pragma unroll
      for (int k = 4; k < 8; ++k) ld(7, k);
      if (sp) WAITV(0); else WAITV(8);
      READY(pu); READY(pw);
      STAMP(14);
      bar();
      if (!__all(pu.x == (u32)tag && pu.y == (u32)(tag >> 32))) poll_flags(ws, WS_UFLAG + xcd * 256, 32, tag, lane);
      u32x4 uc[2]; u32x2 up[2];
#pragma unroll
      for (int k = 0; k < 2; ++k) {
        const u32 o = WS_UT + (u32)(ch0 + c) * 4096 + (u32)(n + 128 * k) * 16;
        uc[k] = ald128(rsw, o);
        up[k] = ald64(rsw, o - 8);
      }
      WAITV(4);
      STAMP(15);
      bar();
      if (t < 192) ldsw<u32x4>(lds, L_UC + (t / 24) * UC_B + (t % 24) * 16, u32x4{0, 0, 0, 0});
      WAITV(0);
      READY(uc[0]); READY(uc[1]); READY(up[0]); READY(up[1]);
#pragma unroll
      for (int k = 0; k < 2; ++k) {
        const int m = n + 128 * k;
        u32 p0 = up[k].x, p1 = up[k].y;
        if (m == 0) { p0 = 0; p1 = 0; }
        const u32 c0 = uc[k].x, c1 = uc[k].y, c2 = uc[k].z, c3 = uc[k].w;
        const u32 s0 = __builtin_amdgcn_alignbit(p1, p0, 16), s1 = __builtin_amdgcn_alignbit(c0, p1, 16), s2 = __builtin_amdgcn_alignbit(c1, c0, 16);
        const u32 s3 = __builtin_amdgcn_alignbit(c2, c1, 16), s4 = __builtin_amdgcn_alignbit(c3, c2, 16);
        const u32 ob = L_UC + c * UC_CH + 384 + m * 16;
        ldsw<u32x4>(lds, ob, u32x4{s0, s1, s2, s3});
        ldsw<u32x4>(lds, ob + UC_B, u32x4{p1, c0, c1, c2});
        ldsw<u32x4>(lds, ob + 2 * UC_B, u32x4{s1, s2, s3, s4});
        ldsw<u32x4>(lds, ob + 3 * UC_B, uc[k]);
      }
      if (!__all(pw.x == (u32)tag && pw.y == (u32)(tag >> 32))) poll_flags(ws, WS_WFLAG + ct * 32, 4, tag, lane);
      STAMP(3);
      bar();
    };
    if (spare) run(std::true_type{}); else run(std::false_type{});
    STAMP(4);
#pragma unroll
    for (int i = 0; i < 7; ++i)
#pragma unroll
      for (int db = 0; db < 4; ++db) {
        wf2[i][db] = ld_sc1(rsw, lane * 16, WS_W2F + (u32)((4 * ct + db) * 56 + 7 * w + i) * 1024);
      }
    bar();
    STAMP(5);
    auto conv = [&](auto HH) {
      constexpr int h = decltype(HH)::value;
      f32x4 cacc[8];
      conv_wave<h>(lds, c, lane, cacc);
      STAMP(6);
#pragma unroll
      for (int i = 0; i < 4; ++i) ldsw<f32x4>(lds, L_RED + aw * 4096 + i * 1024 + lane * 16, cacc[4 * (1 - h) + i]);
      asm volatile("s_waitcnt lgkmcnt(0)" ::: "memory");
      if (lane == 0) ldsw<u32>(lds, L_MISC + 32 + aw * 4, 1u);
#pragma unroll 1
      for (u32 spin = 0; spin < SPIN_CAP; ++spin) {
        u32 v = __hip_atomic_load((__attribute__((address_space(3))) u32*)(lds + L_MISC + 32 + (aw ^ 1) * 4), __ATOMIC_RELAXED, __HIP_MEMORY_SCOPE_WORKGROUP);
        if (__builtin_amdgcn_readfirstlane(v)) break;
      }
      asm volatile("" ::: "memory");
      const int r = lane & 15, q = lane >> 4;
#pragma unroll
      for (int i = 0; i < 4; ++i) {
        const f32x4 y = cacc[4 * h + i] + ldsr<f32x4>(lds, L_RED + (aw ^ 1) * 4096 + i * 1024 + lane * 16);
        const int l = 256 * (4 * h + i) + 64 * q + 4 * r;
        const u32x2 uu = ldsr<u32x2>(lds, L_UC + c * UC_CH + 3 * UC_B + 384 + l * 2);
        const float a0 = rbf(y[0]) + rbf(bf2f(uu.x & 0xffffu) * dsk), a1 = rbf(y[1]) + rbf(__uint_as_float(uu.x & 0xffff0000u) * dsk);
        const float a2 = rbf(y[2]) + rbf(bf2f(uu.y & 0xffffu) * dsk), a3 = rbf(y[3]) + rbf(__uint_as_float(uu.y & 0xffff0000u) * dsk);
        __builtin_amdgcn_raw_buffer_store_b64(u32x2{pk2(a0, a1), pk2(a2, a3)}, rsw, (u32)l * 2, WS_AT + (u32)(ch0 + c) * 4096, AUX_SC1);
      }
    };
    if (aw & 1) conv(std::integral_constant<int, 1>{}); else conv(std::integral_constant<int, 0>{});
    WAITV(0);
    if (last_arrival(lds, L_MISC + 20, 4, lane) && lane == 0) flag_store(ws, WS_AFLAG + (32 * xcd + idx) * 8, tag);
    STAMP(7);
    poll_flags(ws, WS_HFLAG + (rb >> 1) * 128, 14, tag, lane);
  }
  {
    STAMP(16);
    s16x8 hf[3][7];
    const u32 hb = WS_HF + (u32)(4 * rb * 56 + 7 * w) * 1024;
#pragma unroll
    for (int k = 0; k < 14; ++k) hf[k / 7][k % 7] = ld_sc1(rsw, lane * 16, hb + ((k / 7) * 56 + k % 7) * 1024);
    const int fr = lane & 15, fg = lane >> 4;
    float gl[4];
    auto finalize = [&](int lb) {
      const int dq = 4 * (w & 3) + fg;
      f32x4 s = ldsr<f32x4>(lds, lb * PART_LB + fr * 272 + dq * 16);
#pragma unroll
      for (int k = 1; k < 8; ++k) s += ldsr<f32x4>(lds, lb * PART_LB + k * PART_W + fr * 272 + dq * 16);
      const int q4 = (lane >> 2) & 3, p = lane & 3;
      const u32x2 av = __builtin_bit_cast(u32x2, ldstr(lds, L_AT + (4 * dq + q4) * 160 + (16 * lb + 4 * p) * 2));
      const float gg = gl[lb], og = rbf(1.f - gg);
      const float a0 = bf2f(av.x & 0xffffu), a1 = __uint_as_float(av.x & 0xffff0000u), a2 = bf2f(av.y & 0xffffu), a3 = __uint_as_float(av.y & 0xffff0000u);
      const float o0 = rbf(gg * a0) + rbf(og * rbf(s[0])), o1 = rbf(gg * a1) + rbf(og * rbf(s[1]));
      const float o2 = rbf(gg * a2) + rbf(og * rbf(s[2])), o3 = rbf(gg * a3) + rbf(og * rbf(s[3]));
      *(u32x2*)(out + (size_t)(64 * rb + 16 * lb + fr) * SD + 64 * ct + 4 * dq) = u32x2{pk2(o0, o1), pk2(o2, o3)};
    };
    s16x8 areg;
#pragma unroll
    for (int lb = 0; lb < 4; ++lb) {
      f32x4 acc[4];
#pragma unroll
      for (int i = 0; i < 7; ++i) {
        if (lb < 2) hf[(lb + 2) % 3][i] = ld_sc1(rsw, lane * 16, hb + ((lb + 2) * 56 + i) * 1024);
#pragma unroll
        for (int db = 0; db < 4; ++db) acc[db] = mfma(wf2[i][db], hf[lb % 3][i], i == 0 ? f32x4{0, 0, 0, 0} : acc[db]);
        SB();
      }
      STAMP(17 + 2 * lb);
      if (lb == 3) { if (w < 4) finalize(0); else finalize(1); }
#pragma unroll
      for (int db = 0; db < 4; ++db) ldsw<f32x4>(lds, lb * PART_LB + w * PART_W + fr * 272 + db * 64 + fg * 16, acc[db]);
      if (lb == 1) {
        if (w == 0) { poll_flags(ws, WS_AFLAG + ct * 256, 32, tag, lane); poll_flags(ws, WS_GFLAG + rb * 8, 1, tag, lane); }
      }
      if (lb == 2) ldsw<s16x8>(lds, L_AT + (tid >> 3) * 160 + (tid & 7) * 16, areg);
      bar();
      STAMP(18 + 2 * lb);
      if (lb == 1) {
        areg = ld_sc1(rsw, (u32)(64 * ct + (tid >> 3)) * 4096 + (u32)(64 * rb) * 2 + (tid & 7) * 16, WS_AT);
#pragma unroll
        for (int k = 0; k < 4; ++k) gl[k] = __uint_as_float(__builtin_amdgcn_raw_buffer_load_b32(rsw, (u32)(64 * rb + 16 * k + fr) * 4, WS_GATE, AUX_SC1));
      }
    }
    if (w < 4) finalize(2); else finalize(3);
    STAMP(25);
  }
  STAMP_FLUSH();
}

extern "C" void kernel_launch(void* const* d_in, const int* in_sizes, int n_in, void* d_out, int out_size, void* d_ws,
                              size_t ws_size, hipStream_t stream) {
  (void)hipFuncSetAttribute((const void*)fused, hipFuncAttributeMaxDynamicSharedMemorySize, LDS_BYTES);
  fused<<<256, 512, LDS_BYTES, stream>>>((const u16*)d_in[0], (const u16*)d_in[1], (const u16*)d_in[2], (const u16*)d_in[3],
      (const u16*)d_in[4], (const u16*)d_in[5], (const u16*)d_in[6], (const u16*)d_in[7], (const u16*)d_in[8],
      (const u16*)d_in[10], (const u16*)d_in[11], (const u16*)d_in[12], (u16*)d_out, (char*)d_ws);
}
```

```cpp
#include <hip/hip_runtime.h>
#include <stdint.h>
#include <type_traits>

typedef unsigned short u16;
typedef unsigned int u32;
typedef unsigned long long u64;
typedef short s16x4 __attribute__((ext_vector_type(4)));
typedef short s16x8 __attribute__((ext_vector_type(8)));
typedef float f32x2 __attribute__((ext_vector_type(2)));
typedef float f32x4 __attribute__((ext_vector_type(4)));
typedef u32 u32x2 __attribute__((ext_vector_type(2)));
typedef u32 u32x4 __attribute__((ext_vector_type(4)));
typedef __bf16 bf16x2 __attribute__((ext_vector_type(2)));
typedef __attribute__((address_space(3))) char lds_char;
typedef __attribute__((address_space(1))) u64 g_u64;
typedef __amdgpu_buffer_rsrc_t rsrc_t;

#define STAGE 9
#define CPRIO 2
#define PACE 0

constexpr int SL = 2048, SD = 512, SN = 128, SF = 1792;

constexpr u32 WS_EPOCH = 0;
constexpr u32 WS_HFLAG = 256;
constexpr u32 WS_UFLAG = 4096;
constexpr u32 WS_WFLAG = 8192;
constexpr u32 WS_GFLAG = 8704;
constexpr u32 WS_AFLAG = 10240;
constexpr u32 WS_GATE = 16384;
constexpr u32 WS_TS = 32768;
constexpr u32 WS_HF = 1u << 20;
constexpr u32 WS_W2F = WS_HF + 128u * 56u * 1024u;
constexpr u32 WS_UT = WS_W2F + 32u * 56u * 1024u;
constexpr u32 WS_AT = WS_UT + 512u * 4096u;
constexpr u32 WS_USED = WS_AT + 512u * 4096u;

constexpr u32 L_TAB = 98304;
constexpr u32 TAB_ROW = 528, TAB_CH = 44 * TAB_ROW;
constexpr u32 L_UC = 98304;
constexpr u32 UC_B = 4640, UC_CH = 4 * UC_B;
constexpr u32 L_R = 144768;
constexpr u32 R_CH = 4224;
constexpr u32 L_STG = 153216;
constexpr u32 L_MISC = 161408;
constexpr u32 PART_W = 16 * 272, PART_LB = 8 * PART_W;
constexpr u32 L_AT = 139264;
constexpr u32 L_RED = 73728;
constexpr u32 LDS_BYTES = 163840;

constexpr int AUX_SC1 = 16;
constexpr u32 SPIN_CAP = 1u << 14;
constexpr float LOG2E = 1.4426950408889634f;

__device__ __forceinline__ float bf2f(u32 v) { return __uint_as_float(v << 16); }
__device__ __forceinline__ u32 pk2(float lo, float hi) { bf16x2 p = {(__bf16)lo, (__bf16)hi}; return __builtin_bit_cast(u32, p); }
__device__ __forceinline__ float rbf(float v) { return __uint_as_float(pk2(v, v) << 16); }
__device__ __forceinline__ rsrc_t mk(const void* p, u32 n) { return __builtin_amdgcn_make_buffer_rsrc((void*)p, (short)0, (int)n, 0x00020000); }
__device__ __forceinline__ void bar() { asm volatile("s_waitcnt lgkmcnt(0)\n\ts_barrier" ::: "memory"); }
#define WAITV(n) asm volatile("s_waitcnt vmcnt(" #n ")" ::: "memory")
#define SB() __builtin_amdgcn_sched_barrier(0)
__device__ __forceinline__ void glds(rsrc_t rs, u32 dst, u32 voff, u32 soff) {
  asm volatile("s_mov_b32 m0, %0\n\ts_nop 0\n\tbuffer_load_dwordx4 %1, %2, %3 offen lds" :: "s"(dst), "v"(voff), "s"(rs), "s"(soff) : "memory");
}
__device__ __forceinline__ void glds2(rsrc_t rs, u32 dbase, int dimm, u32 voff, u32 sbase, int simm) {
  u32 so;
  asm volatile("s_add_u32 m0, %1, %2\n\ts_add_u32 %0, %5, %6\n\tbuffer_load_dwordx4 %3, %4, %0 offen lds"
               : "=&s"(so) : "s"(dbase), "i"(dimm), "v"(voff), "s"(rs), "s"(sbase), "i"(simm) : "memory", "scc");
}
__device__ __forceinline__ u32x4 ald128(rsrc_t rs, u32 voff) { u32x4 v; asm volatile("buffer_load_dwordx4 %0, %1, %2, 0 offen sc1" : "=v"(v) : "v"(voff), "s"(rs) : "memory"); return v; }
__device__ __forceinline__ u32x2 ald64(rsrc_t rs, u32 voff) { u32x2 v; asm volatile("buffer_load_dwordx2 %0, %1, %2, 0 offen sc1" : "=v"(v) : "v"(voff), "s"(rs) : "memory"); return v; }
#define READY(v) asm volatile("" : "+v"(v))
template <class T> __device__ __forceinline__ T ldsr(lds_char* b, u32 off) { return *(const __attribute__((address_space(3))) T*)(b + off); }
template <class T> __device__ __forceinline__ void ldsw(lds_char* b, u32 off, T v) { *(__attribute__((address_space(3))) T*)(b + off) = v; }
__device__ __forceinline__ s16x4 ldstr(lds_char* b, u32 off) {
  return __builtin_amdgcn_ds_read_tr16_b64_v4i16((__attribute__((address_space(3))) s16x4*)(b + off));
}
__device__ __forceinline__ s16x8 cat(s16x4 a, s16x4 b) { return __builtin_shufflevector(a, b, 0, 1, 2, 3, 4, 5, 6, 7); }
__device__ __forceinline__ f32x4 mfma(s16x8 a, s16x8 b, f32x4 c) { return __builtin_amdgcn_mfma_f32_16x16x32_bf16(a, b, c, 0, 0, 0); }
__device__ __forceinline__ s16x8 ld_sc1(rsrc_t rs, u32 voff, u32 soff) {
  return __builtin_bit_cast(s16x8, __builtin_amdgcn_raw_buffer_load_b128(rs, voff, soff, AUX_SC1));
}

__device__ __forceinline__ void flag_store(char* ws, u32 off, u64 tag) {
  __hip_atomic_store((g_u64*)(ws + off), tag, __ATOMIC_RELAXED, __HIP_MEMORY_SCOPE_AGENT);
}
__device__ __forceinline__ void poll_flags(char* ws, u32 off, int n, u64 tag, int lane) {
  g_u64* p = (g_u64*)(ws + off) + (lane < n ? lane : 0);
#pragma unroll 1
  for (u32 spin = 0; spin < SPIN_CAP; ++spin) {
    u64 v = __hip_atomic_load(p, __ATOMIC_RELAXED, __HIP_MEMORY_SCOPE_AGENT);
    if (__all(v == tag)) break;
    __builtin_amdgcn_s_sleep(1);
  }
  asm volatile("" ::: "memory");
}
__device__ __forceinline__ bool last_arrival(lds_char* lds, u32 off, u32 n, int lane) {
  u32 old = 0;
  if (lane == 0) old = __hip_atomic_fetch_add((__attribute__((address_space(3))) u32*)(lds + off), 1u, __ATOMIC_RELAXED, __HIP_MEMORY_SCOPE_WORKGROUP);
  old = __builtin_amdgcn_readfirstlane(old);
  return old == n - 1;
}
__device__ __forceinline__ u64 read_tag(lds_char* lds) {
  u32x2 t = ldsr<u32x2>(lds, L_MISC);
  return ((u64)(u32)__builtin_amdgcn_readfirstlane(t.y) << 32) | (u32)__builtin_amdgcn_readfirstlane(t.x);
}

#define STAMP(i) do {} while (0)
#define BARS(i) bar()
#define STAMP_DECL
#define STAMP_FLUSH() do {} while (0)

struct cf { float re, im; };
__device__ __forceinline__ cf cmul(cf a, cf b) {
  float t1, t2; cf r;
  asm("v_mul_f32 %0, %1, %2" : "=v"(t1) : "v"(a.im), "v"(b.im));
  asm("v_mul_f32 %0, %1, %2" : "=v"(t2) : "v"(a.im), "v"(b.re));
  asm("v_fma_f32 %0, %1, %2, -%3" : "=v"(r.re) : "v"(a.re), "v"(b.re), "v"(t1));
  asm("v_fma_f32 %0, %1, %2, %3" : "=v"(r.im) : "v"(a.re), "v"(b.im), "v"(t2));
  return r;
}
__device__ __forceinline__ cf zpow(float l2, float rev, float m) {
  float e = __builtin_amdgcn_exp2f(l2 * m);
  float a = __builtin_amdgcn_fractf(rev * m);
  cf r; r.re = e * __builtin_amdgcn_cosf(a); r.im = e * __builtin_amdgcn_sinf(a);
  return r;
}

template <int SEG> __device__ __forceinline__ void kgen_job(lds_char* lds, int c, int lane) {
  const int j = lane & 15, kq = lane >> 4;
  const int rowA = SEG == 0 ? 16 + j : (SEG == 1 ? 32 : (SEG == 2 ? 16 : 28)) + 3 * (j >> 2) + ((j & 3) < 2 ? (j & 3) : 2);
  const u32 pa = L_TAB + c * TAB_CH + rowA * TAB_ROW + kq * 16, pb = L_TAB + c * TAB_CH + j * TAB_ROW + kq * 16;
  f32x4 acc = {0, 0, 0, 0};
#pragma unroll
  for (int s = 0; s < 8; ++s) acc = mfma(ldsr<s16x8>(lds, pa + s * 64), ldsr<s16x8>(lds, pb + s * 64), acc);
  const u32 rb = L_R + c * R_CH;
  const int i = j, q = kq;
  if (SEG == 0) {
    ldsw<u32x2>(lds, rb + 2 * (2044 - 16 * i - 4 * q), u32x2{pk2(acc[3], acc[2]), pk2(acc[1], acc[0])});
  } else if (SEG == 1) {
    ldsw<u32x2>(lds, rb + 2 * (2044 - 256 - 16 * i - 4 * q), u32x2{pk2(acc[2], acc[1]), pk2(acc[0], acc[0])});
  } else if (SEG == 2) {
    const int lag0 = 512 + 256 * (q >> 1) + 16 * i + 8 * (q & 1);
    ldsw<u32x4>(lds, rb + 2 * (2040 - lag0), u32x4{pk2(acc[2], acc[2]), pk2(acc[1], acc[1]), pk2(acc[1], acc[0]), pk2(acc[0], acc[0])});
  } else {
    const int lag0 = 1024 + 256 * q + 16 * i;
    const u32 x00 = pk2(acc[0], acc[0]), x11 = pk2(acc[1], acc[1]), x22 = pk2(acc[2], acc[2]), x10 = pk2(acc[1], acc[0]);
    ldsw<u32x4>(lds, rb + 2 * (2032 - lag0), u32x4{x22, x22, x11, x11});
    ldsw<u32x4>(lds, rb + 2 * (2040 - lag0), u32x4{x11, x10, x00, x00});
  }
}

constexpr int CONV_LEAD = 10;
template <int H> __device__ __forceinline__ void conv_wave(lds_char* lds, int c, int lane, f32x4 (&acc)[8]) {
  const int r = lane & 15, kq = lane >> 4;
  u32 ub = L_UC + c * UC_CH + (lane & 3) * UC_B + 2 * (192 + 64 * ((lane >> 2) & 3) + 32 + 8 * kq) - 64 * (4 * H + 3);
  u32 kb0 = L_R + c * R_CH + 2 * (2044 - 4 * r + 32 + 8 * kq) - 64 * (4 * H + 3) - 512 * 7, kb1 = kb0 + 2048;
  asm("" : "+v"(ub)); asm("" : "+v"(kb0)); asm("" : "+v"(kb1));
  s16x8 kf[4][8], uf[4][8];
#pragma unroll
  for (int i = 0; i < 8; ++i) acc[i] = f32x4{0, 0, 0, 0};
#pragma unroll
  for (int sl = 0; sl < 4; ++sl)
#pragma unroll
    for (int j = 0; j < 8; ++j)
#pragma unroll
      for (int d = 0; d <= j; ++d) {
        const int g = 36 * sl + j * (j + 1) / 2 + d;
#pragma unroll
        for (int s2 = 0; s2 < 4; ++s2)
#pragma unroll
          for (int j2 = 0; j2 < 8; ++j2) {
            const int first = 36 * s2 + j2 * (j2 + 1) / 2 - CONV_LEAD;
            if ((first < 0 ? 0 : first) == g) {
              const u32 ko = (j2 < 4 ? kb1 : kb0) + 64 * (3 - s2) + 512 * ((7 - j2) & 3);
              kf[s2][j2] = cat(ldsr<s16x4>(lds, ko), ldsr<s16x4>(lds, ko + 8));
              uf[s2][7 - j2] = ldsr<s16x8>(lds, ub + 64 * (3 - s2) + 512 * (7 - j2));
            }
          }
        acc[7 - j + d] = mfma(uf[sl][7 - j], kf[sl][d], acc[7 - j + d]);
        SB();
      }
}

__global__ __launch_bounds__(512, 2) void fused(const u16* __restrict__ x, const u16* __restrict__ Are, const u16* __restrict__ Aim,
    const u16* __restrict__ Bre, const u16* __restrict__ Bim, const u16* __restrict__ Cre, const u16* __restrict__ Cim,
    const u16* __restrict__ logdt, const u16* __restrict__ Dskip, const u16* __restrict__ w1, const u16* __restrict__ w2,
    const u16* __restrict__ gw, u16* __restrict__ out, char* __restrict__ ws) {
  extern __shared__ __attribute__((aligned(16))) char smem[];
  lds_char* lds = (lds_char*)smem;
  const int tid = threadIdx.x, lane = tid & 63;
  const int w = __builtin_amdgcn_readfirstlane(tid >> 6);
  const int b = blockIdx.x, xcd = b & 7, idx = b >> 3;
  const bool spare = idx >= 28;
  const rsrc_t rsx = mk(x, SL * SD * 2), rsw = mk(ws, WS_USED);
  const int panel = 4 * (xcd >> 1) + (spare ? 0 : idx / 7), slice = 7 * (xcd & 1) + (spare ? 0 : idx % 7);
  const int ch0 = 64 * xcd + 2 * idx;
  const int rb = 4 * xcd + (idx >> 3), ct = idx & 7;
  const int sq = 4 * xcd + (idx - 28);
  u64 tag = 0;
  s16x8 wf2[7][4];
  STAMP_DECL;
  STAMP(0);
  if (w == 7) {
    u64 pc; u32 len;
    asm volatile("s_getpc_b64 %0\n.Lfused_beg:\n\ts_mov_b32 %1, .Lfused_end-.Lfused_beg" : "=s"(pc), "=s"(len));
    const rsrc_t rsc = mk((const void*)(pc & ~63ull), len);
    glds(rsc, L_R, lane * 16, idx * 2048);
    glds(rsc, L_R + 1024, lane * 16, idx * 2048 + 1024);
  }

  if (w < 4) {
    const int g = lane >> 4, q4 = (lane >> 2) & 3, p = lane & 3;
    const u32 v128 = (u32)(lane >> 3) * 1024 + (u32)(((lane & 7) >> 1) ^ ((lane >> 4) & 3)) * 32 + (lane & 1) * 16;
#pragma unroll
    for (int i = 0; i < 2; ++i) glds(rsx, L_STG + (w * 2 + i) * 1024, v128, (u32)(64 * idx + 16 * w + 8 * i) * 1024 + xcd * 128);
    if (spare) {
      const rsrc_t rs2 = mk(w2, SF * SD * 2);
#pragma unroll
      for (int i = 0; i < 14; ++i) glds(rs2, (w * 14 + i) * 1024, v128, (u32)(448 * (sq & 3) + (w * 14 + i) * 8) * 1024 + (sq >> 2) * 128);
    }
    float sd[6];
    {
      const int d = ch0 + (w >> 1), o = d * SN + (tid & 127);
      const float dt = rbf(__expf(bf2f(logdt[d])));
      const float ar = dt * bf2f(Are[o]), ai = dt * bf2f(Aim[o]), br = dt * bf2f(Bre[o]), bi = dt * bf2f(Bim[o]);
      const float cr = bf2f(Cre[o]), ci = bf2f(Cim[o]);
      const float l2 = ar * LOG2E, rev = ai * 0.15915494309189535f;
      cf z = zpow(l2, rev, 1.f), z256 = zpow(l2, rev, 256.f);
      sd[0] = z.re; sd[1] = z.im; sd[2] = z256.re; sd[3] = z256.im;
      sd[4] = cr * br - ci * bi; sd[5] = cr * bi + ci * br;
    }
    WAITV(0);
    {
#pragma unroll
      for (int m = 0; m < 2; ++m) {
        const int cb = 2 * m + (g >> 1), r0 = 8 * (g & 1) + q4, r1 = r0 + 4;
        s16x4 lo = ldstr(lds, L_STG + w * 2048 + r0 * 128 + ((cb ^ ((r0 >> 1) & 3)) * 32) + p * 8);
        s16x4 hi = ldstr(lds, L_STG + w * 2048 + r1 * 128 + ((cb ^ ((r1 >> 1) & 3)) * 32) + p * 8);
        u32 off = WS_UT + (u32)(64 * xcd + 16 * cb + (lane & 15)) * 4096 + (u32)(64 * idx + 16 * w + 8 * (g & 1)) * 2;
        __builtin_amdgcn_raw_buffer_store_b128(__builtin_bit_cast(u32x4, cat(lo, hi)), rsw, off, 0, AUX_SC1);
      }
      asm volatile("s_waitcnt lgkmcnt(0)" ::: "memory");
#pragma unroll
      for (int k = 0; k < 6; ++k) ldsw<float>(lds, L_STG + w * 2048 + k * 256 + lane * 4, sd[k]);
    }
    __builtin_amdgcn_s_setprio(CPRIO);
    BARS(1);
    tag = read_tag(lds);
    STAMP(2);
    if (!spare) {
      const int wf = w & 1, wl = w >> 1;
      const int r = lane & 15;
      const u32 xb = wl * 8192 + r * 128 + (((g ^ (r >> 1)) & 7) << 4);
      const int swz = q4 | ((g & 1) << 2);
      u32 wb[4];
#pragma unroll
      for (int ft = 0; ft < 4; ++ft) wb[ft] = 16384 + (8 * g + q4) * 256 + (((4 * wf + ft) ^ swz) << 5) + p * 8;
      f32x4 acc[4][4];
#pragma unroll
      for (int i = 0; i < 4; ++i)
#pragma unroll
        for (int j = 0; j < 4; ++j) acc[i][j] = f32x4{0, 0, 0, 0};
      s16x8 fa[2][4], fb[2][4];
      auto rdA = [&](int j, int ft) { u32 o = ((j >> 1) % 3) * 32768 + (j & 1) * 8192; return cat(ldstr(lds, wb[ft] + o), ldstr(lds, wb[ft] + o + 1024)); };
      auto rdB = [&](int j, int lt) { u32 o = ((j >> 1) % 3) * 32768 + lt * 2048; return ldsr<s16x8>(lds, (xb ^ ((j & 1) << 6)) + o); };
#pragma unroll
      for (int i = 0; i < 4; ++i) { fa[0][i] = rdA(0, i); fb[0][i] = rdB(0, i); }
#pragma unroll
      for (int j = 0; j < 16; ++j) {
        const int c = j & 1, n = c ^ 1;
        if (j == 3) WAITV(0);
        if (c == 1 && j < 15) BARS(8 + (j >> 1));
        if (j == 3 && w == 0 && lane == 0) flag_store(ws, WS_UFLAG + (xcd * 32 + idx) * 8, tag);
        SB();
#pragma unroll
        for (int m = 0; m < 16; ++m) {
          const int ft = m >> 2, lt = m & 3;
          acc[ft][lt] = mfma(fa[c][ft], fb[c][lt], acc[ft][lt]);
          if (j < 15) {
            if (m < 4) fb[n][m] = rdB(j + 1, m);
            else if (m < 8) fa[n][m - 4] = rdA(j + 1, m - 4);
          }
          SB();
        }
      }
      BARS(3);
      STAMP(4);
#pragma unroll
      for (int m = 0; m < 2; ++m)
#pragma unroll
        for (int lt = 0; lt < 4; ++lt) {
          f32x2 v[4], t[4];
          v[0] = acc[2 * m][lt].xy; v[1] = acc[2 * m][lt].zw; v[2] = acc[2 * m + 1][lt].xy; v[3] = acc[2 * m + 1][lt].zw;
#pragma unroll
          for (int i = 0; i < 4; ++i) t[i] = v[i] * -LOG2E;
          SB();
#pragma unroll
          for (int i = 0; i < 4; ++i) { t[i].x = __builtin_amdgcn_exp2f(t[i].x); t[i].y = __builtin_amdgcn_exp2f(t[i].y); }
          SB();
#pragma unroll
          for (int i = 0; i < 4; ++i) t[i] = t[i] + 1.f;
          SB();
#pragma unroll
          for (int i = 0; i < 4; ++i) { t[i].x = __builtin_amdgcn_rcpf(t[i].x); t[i].y = __builtin_amdgcn_rcpf(t[i].y); }
          SB();
          u32x4 o;
#pragma unroll
          for (int i = 0; i < 4; ++i) { f32x2 s = v[i] * t[i]; o[i] = pk2(s.x, s.y); }
          u32 off = WS_HF + (u32)((panel * 8 + 4 * wl + lt) * 56 + slice * 4 + 2 * wf + m) * 1024;
          __builtin_amdgcn_raw_buffer_store_b128(o, rsw, lane * 16 + off, 0, AUX_SC1);
          SB();
        }
      BARS(5);
      __builtin_amdgcn_s_setprio(0);
      WAITV(0);
      STAMP(6);
      if (last_arrival(lds, L_MISC + 16, 4, lane) && lane == 0) flag_store(ws, WS_HFLAG + (panel * 16 + slice) * 8, tag);
    } else {
#pragma unroll
      for (int si = 0; si < 14; ++si) {
        if (si == 5) bar();
        if (si == 10) {
          WAITV(10); bar();
          if (w == 0 && lane == 0) flag_store(ws, WS_UFLAG + (xcd * 32 + idx) * 8, tag);
        }
        const int r0 = 32 * si + 4 * g + q4, r1 = r0 + 16;
        s16x4 lo = ldstr(lds, r0 * 128 + ((w ^ ((r0 >> 1) & 3)) * 32) + p * 8);
        s16x4 hi = ldstr(lds, r1 * 128 + ((w ^ ((r1 >> 1) & 3)) * 32) + p * 8);
        u32 off = WS_W2F + (u32)((4 * (sq >> 2) + w) * 56 + 14 * (sq & 3) + si) * 1024;
        __builtin_amdgcn_raw_buffer_store_b128(__builtin_bit_cast(u32x4, cat(lo, hi)), rsw, lane * 16 + off, 0, AUX_SC1);
      }
      bar();
      WAITV(0);
      bar();
      if (w == 0 && lane == 0) flag_store(ws, WS_WFLAG + sq * 8, tag);
#pragma unroll
      for (int i = 5; i <= 8; ++i) bar();
      __builtin_amdgcn_s_setprio(0);
      STAMP(4);
      {
        const u32x4 gv = *(const u32x4*)(gw + lane * 8);
        float keep = 0.f;
#pragma unroll
        for (int h = 0; h < 2; ++h) {
          u32x4 xr[8];
#pragma unroll
          for (int i = 0; i < 8; ++i) xr[i] = *(const u32x4*)(x + (size_t)(64 * sq + 16 * w + 8 * h + i) * SD + lane * 8);
#pragma unroll
          for (int i = 0; i < 8; ++i) {
            float s = 0.f;
#pragma unroll
            for (int k = 0; k < 4; ++k) {
              u32 pr = pk2(bf2f(xr[i][k] & 0xffffu) * bf2f(gv[k] & 0xffffu), __uint_as_float(xr[i][k] & 0xffff0000u) * __uint_as_float(gv[k] & 0xffff0000u));
              s += __uint_as_float(pr << 16) + __uint_as_float(pr & 0xffff0000u);
            }
#pragma unroll
            for (int o = 32; o >= 1; o >>= 1) s += __shfl_xor(s, o);
            s = rbf(s);
            s = rbf(1.f / (1.f + __expf(-s)));
            if (lane == 8 * h + i) keep = s;
          }
        }
        if (lane < 16) __builtin_amdgcn_raw_buffer_store_b32(__float_as_uint(keep), rsw, (u32)(64 * sq + 16 * w + lane) * 4, WS_GATE, AUX_SC1);
      }
      STAMP(5);
      bar();
      WAITV(0);
      if (last_arrival(lds, L_MISC + 16, 4, lane) && lane == 0) flag_store(ws, WS_GFLAG + sq * 8, tag);
      if (sq == 0 && w == 0) {
#pragma unroll
        for (int i = 0; i < 4; ++i) poll_flags(ws, WS_UFLAG + i * 512, 64, tag, lane);
        if (lane == 0) flag_store(ws, WS_EPOCH, tag);
      }
    }
#pragma unroll
    for (int i = 0; i < 7; ++i)
#pragma unroll
      for (int db = 0; db < 4; ++db) wf2[i][db] = ld_sc1(rsw, lane * 16, WS_W2F + (u32)((4 * ct + db) * 56 + 7 * w + i) * 1024);
    poll_flags(ws, WS_HFLAG + (rb >> 1) * 128, 14, tag, lane);
    STAMP(7);
  } else {
    const int aw = w - 4, t = tid - 256;
    const int c = aw >> 1, n = t & 127;
    const rsrc_t rs1 = mk(w1, SD * SF * 2);
    const u32 vx = (u32)(lane >> 3) * 1024 + (u32)((lane & 7) ^ (lane >> 4)) * 16;
    const u32 cw = (u32)((((lane & 15) >> 1) ^ (lane >> 4)) << 5) | ((lane & 1) << 4);
    const u32 vx0 = vx, vx1 = vx ^ 64, vw0 = (u32)(lane >> 4) * 3584 + cw, vw1 = (u32)(lane >> 4) * 3584 + (cw ^ 128);
    const u32 sA = (u32)aw * 4096, sX = (u32)panel * 131072 + (u32)aw * 32768, sW = (u32)aw * (16 * 3584) + (u32)slice * 256;
    float dsk;
    auto run = [&](auto SPARE) {
      constexpr bool sp = decltype(SPARE)::value;
      auto ld = [&](int s, int k) {
        if (sp) return;
        const int i = k >> 1, d = (s % 3) * 32768;
        if (k & 1) glds2(rs1, sA, d + 16384 + i * 1024, (i & 2) ? vw1 : vw0, sW, (64 * s + 4 * i) * 3584);
        else glds2(rsx, sA, d + i * 1024, (i & 1) ? vx1 : vx0, sX, i * 8192 + s * 128);
      };
      u32x2 epv = ald64(rsw, WS_EPOCH);
      const u32 dskb = Dskip[ch0 + c];
#pragma unroll
      for (int k = 0; k < 16; ++k) ld(k >> 3, k & 7);
      if (sp) WAITV(0); else WAITV(16);
      READY(epv);
      dsk = bf2f(dskb);
      if (aw == 0 && lane == 0) {
        const u64 ep = (((u64)epv.y << 32) | epv.x) + 1;
        ldsw<u32x2>(lds, L_MISC, u32x2{(u32)ep, (u32)(ep >> 32)});
        ldsw<u32x4>(lds, L_MISC + 16, u32x4{0, 0, 0, 0});
        ldsw<u32x4>(lds, L_MISC + 32, u32x4{0, 0, 0, 0});
      }
      WAITV(8);
      BARS(1);
      tag = read_tag(lds);
      STAMP(2);
      const u32 tb = L_TAB + c * TAB_CH + n * 4;
      auto put = [&](int row, cf v) { ldsw<u32>(lds, tb + row * TAB_ROW, pk2(v.re, v.im)); };
      cf z, z256, cc;
      z.re = ldsr<float>(lds, L_STG + aw * 2048 + 0 * 256 + lane * 4); z.im = ldsr<float>(lds, L_STG + aw * 2048 + 1 * 256 + lane * 4);
      z256.re = ldsr<float>(lds, L_STG + aw * 2048 + 2 * 256 + lane * 4); z256.im = ldsr<float>(lds, L_STG + aw * 2048 + 3 * 256 + lane * 4);
      cc.re = ldsr<float>(lds, L_STG + aw * 2048 + 4 * 256 + lane * 4); cc.im = ldsr<float>(lds, L_STG + aw * 2048 + 5 * 256 + lane * 4);
      cf ev[9];
      {
        ld(2, 0);
        cf pz = z;
        put(16, cf{1.f, 0.f}); put(17, z);
        ld(2, 1);
#pragma unroll
        for (int j = 2; j <= 16; ++j) {
          pz = cmul(pz, z);
          if (j < 16) put(16 + j, pz);
          if ((j & 1) == 0) ev[j >> 1] = pz;
          if ((j & 1) && j < 15) ld(2, (j + 1) >> 1);
        }
      }
      WAITV(8); BARS(9);
      {
        cf pp = cc;
        ld(3, 0);
        put(0, cf{pp.re, -pp.im});
#pragma unroll
        for (int i = 1; i < 16; ++i) {
          pp = cmul(pp, ev[8]); put(i, cf{pp.re, -pp.im});
          if (i & 1) { if (i < 15) ld(3, (i + 1) >> 1); }
        }
      }
      WAITV(8); BARS(10);
      cf z512, z768;
      {
        ld(4, 0);
        put(32, z256);
#pragma unroll
        for (int k = 1; k <= 8; ++k) {
          cf v = cmul(z256, ev[k]);
          if (k & 1) put(32 + 3 * (k >> 1) + 1, v);
          else { put(32 + 3 * ((k >> 1) - 1) + 2, v); if (k < 8) put(32 + 3 * (k >> 1), v); }
          if (k < 8) ld(4, k);
        }
        z512 = cmul(z256, z256); z768 = cmul(z512, z256);
      }
      WAITV(8); BARS(11);
      u32 q2[10];
      {
        ld(5, 0);
        if (aw & 1) kgen_job<1>(lds, c, lane); else kgen_job<0>(lds, c, lane);
#pragma unroll
        for (int a = 0; a < 2; ++a) {
          cf base = a ? z768 : z512;
          q2[5 * a] = pk2(base.re, base.im);
#pragma unroll
          for (int k = 1; k <= 4; ++k) {
            cf v = cmul(base, ev[2 * k]); q2[5 * a + k] = pk2(v.re, v.im);
            SB();
            if (4 * a + k < 8) ld(5, 4 * a + k);
          }
        }
      }
      WAITV(8); BARS(12);
      {
        ld(6, 0);
#pragma unroll
        for (int a = 0; a < 2; ++a) {
          const u32 o = tb + (16 + 6 * a) * TAB_ROW;
          ldsw<u32>(lds, o, q2[5 * a]); ldsw<u32>(lds, o + TAB_ROW, q2[5 * a + 1]); ldsw<u32>(lds, o + 2 * TAB_ROW, q2[5 * a + 2]);
          ldsw<u32>(lds, o + 3 * TAB_ROW, q2[5 * a + 2]); ldsw<u32>(lds, o + 4 * TAB_ROW, q2[5 * a + 3]); ldsw<u32>(lds, o + 5 * TAB_ROW, q2[5 * a + 4]);
          ld(6, 1 + a);
        }
        cf base = cmul(z512, z512);
#pragma unroll
        for (int q = 0; q < 4; ++q) {
          cf v8 = cmul(base, ev[4]), v16 = cmul(base, ev[8]);
          put(28 + 3 * q, base); put(28 + 3 * q + 1, v8); put(28 + 3 * q + 2, v16);
          if (q < 3) base = cmul(base, z256);
          ld(6, 3 + q);
        }
        ld(6, 7);
        if (t < 64) ldsw<u32>(lds, L_R + (t >> 5) * R_CH + 4096 + (t & 31) * 4, 0u);
      }
      WAITV(8); BARS(13);
      u32x2 pu = ald64(rsw, WS_UFLAG + (xcd * 32 + (lane & 31)) * 8);
      u32x2 pw = ald64(rsw, WS_WFLAG + (ct * 4 + (lane & 3)) * 8);
#pragma unroll
      for (int k = 0; k < 4; ++k) ld(7, k);
      if (aw & 1) kgen_job<3>(lds, c, lane); else kgen_job<2>(lds, c, lane);
#pragma unroll
      for (int k = 4; k < 8; ++k) ld(7, k);
      if (sp) WAITV(0); else WAITV(8);
      READY(pu); READY(pw);
      BARS(14);
      if (!__all(pu.x == (u32)tag && pu.y == (u32)(tag >> 32))) poll_flags(ws, WS_UFLAG + xcd * 256, 32, tag, lane);
      u32x4 uc[2]; u32x2 up[2];
#pragma unroll
      for (int k = 0; k < 2; ++k) {
        const u32 o = WS_UT + (u32)(ch0 + c) * 4096 + (u32)(n + 128 * k) * 16;
        uc[k] = ald128(rsw, o);
        up[k] = ald64(rsw, o - 8);
      }
      WAITV(4);
      BARS(15);
      if (t < 192) ldsw<u32x4>(lds, L_UC + (t / 24) * UC_B + (t % 24) * 16, u32x4{0, 0, 0, 0});
      WAITV(0);
      READY(uc[0]); READY(uc[1]); READY(up[0]); READY(up[1]);
#pragma unroll
      for (int k = 0; k < 2; ++k) {
        const int m = n + 128 * k;
        u32 p0 = up[k].x, p1 = up[k].y;
        if (m == 0) { p0 = 0; p1 = 0; }
        const u32 c0 = uc[k].x, c1 = uc[k].y, c2 = uc[k].z, c3 = uc[k].w;
        const u32 s0 = __builtin_amdgcn_alignbit(p1, p0, 16), s1 = __builtin_amdgcn_alignbit(c0, p1, 16), s2 = __builtin_amdgcn_alignbit(c1, c0, 16);
        const u32 s3 = __builtin_amdgcn_alignbit(c2, c1, 16), s4 = __builtin_amdgcn_alignbit(c3, c2, 16);
        const u32 ob = L_UC + c * UC_CH + 384 + m * 16;
        ldsw<u32x4>(lds, ob, u32x4{s0, s1, s2, s3});
        ldsw<u32x4>(lds, ob + UC_B, u32x4{p1, c0, c1, c2});
        ldsw<u32x4>(lds, ob + 2 * UC_B, u32x4{s1, s2, s3, s4});
        ldsw<u32x4>(lds, ob + 3 * UC_B, uc[k]);
      }
      if (!__all(pw.x == (u32)tag && pw.y == (u32)(tag >> 32))) poll_flags(ws, WS_WFLAG + ct * 32, 4, tag, lane);
      BARS(3);
    };
    if (spare) run(std::true_type{}); else run(std::false_type{});
    STAMP(4);
#pragma unroll
    for (int i = 0; i < 7; ++i)
#pragma unroll
      for (int db = 0; db < 4; ++db) {
        wf2[i][db] = ld_sc1(rsw, lane * 16, WS_W2F + (u32)((4 * ct + db) * 56 + 7 * w + i) * 1024);
      }
    bar();
    STAMP(5);
    auto conv = [&](auto HH) {
      constexpr int h = decltype(HH)::value;
      f32x4 cacc[8];
      conv_wave<h>(lds, c, lane, cacc);
      STAMP(6);
#pragma unroll
      for (int i = 0; i < 4; ++i) ldsw<f32x4>(lds, L_RED + aw * 4096 + i * 1024 + lane * 16, cacc[4 * (1 - h) + i]);
      asm volatile("s_waitcnt lgkmcnt(0)" ::: "memory");
      if (lane == 0) ldsw<u32>(lds, L_MISC + 32 + aw * 4, 1u);
#pragma unroll 1
      for (u32 spin = 0; spin < SPIN_CAP; ++spin) {
        u32 v = __hip_atomic_load((__attribute__((address_space(3))) u32*)(lds + L_MISC + 32 + (aw ^ 1) * 4), __ATOMIC_RELAXED, __HIP_MEMORY_SCOPE_WORKGROUP);
        if (__builtin_amdgcn_readfirstlane(v)) break;
      }
      asm volatile("" ::: "memory");
      const int r = lane & 15, q = lane >> 4;
#pragma unroll
      for (int i = 0; i < 4; ++i) {
        const f32x4 y = cacc[4 * h + i] + ldsr<f32x4>(lds, L_RED + (aw ^ 1) * 4096 + i * 1024 + lane * 16);
        const int l = 256 * (4 * h + i) + 64 * q + 4 * r;
        const u32x2 uu = ldsr<u32x2>(lds, L_UC + c * UC_CH + 3 * UC_B + 384 + l * 2);
        const float a0 = rbf(y[0]) + rbf(bf2f(uu.x & 0xffffu) * dsk), a1 = rbf(y[1]) + rbf(__uint_as_float(uu.x & 0xffff0000u) * dsk);
        const float a2 = rbf(y[2]) + rbf(bf2f(uu.y & 0xffffu) * dsk), a3 = rbf(y[3]) + rbf(__uint_as_float(uu.y & 0xffff0000u) * dsk);
        __builtin_amdgcn_raw_buffer_store_b64(u32x2{pk2(a0, a1), pk2(a2, a3)}, rsw, (u32)l * 2, WS_AT + (u32)(ch0 + c) * 4096, AUX_SC1);
      }
    };
    if (aw & 1) conv(std::integral_constant<int, 1>{}); else conv(std::integral_constant<int, 0>{});
    WAITV(0);
    if (last_arrival(lds, L_MISC + 20, 4, lane) && lane == 0) flag_store(ws, WS_AFLAG + (32 * xcd + idx) * 8, tag);
    STAMP(7);
    poll_flags(ws, WS_HFLAG + (rb >> 1) * 128, 14, tag, lane);
  }
  {
    STAMP(16);
    s16x8 hf[3][7];
    const u32 hb = WS_HF + (u32)(4 * rb * 56 + 7 * w) * 1024;
#pragma unroll
    for (int k = 0; k < 14; ++k) hf[k / 7][k % 7] = ld_sc1(rsw, lane * 16, hb + ((k / 7) * 56 + k % 7) * 1024);
    const int fr = lane & 15, fg = lane >> 4;
    float gl[4];
    auto finalize = [&](int lb) {
      const int dq = 4 * (w & 3) + fg;
      f32x4 s = ldsr<f32x4>(lds, lb * PART_LB + fr * 272 + dq * 16);
#pragma unroll
      for (int k = 1; k < 8; ++k) s += ldsr<f32x4>(lds, lb * PART_LB + k * PART_W + fr * 272 + dq * 16);
      const int q4 = (lane >> 2) & 3, p = lane & 3;
      const u32x2 av = __builtin_bit_cast(u32x2, ldstr(lds, L_AT + (4 * dq + q4) * 160 + (16 * lb + 4 * p) * 2));
      const float gg = gl[lb], og = rbf(1.f - gg);
      const float a0 = bf2f(av.x & 0xffffu), a1 = __uint_as_float(av.x & 0xffff0000u), a2 = bf2f(av.y & 0xffffu), a3 = __uint_as_float(av.y & 0xffff0000u);
      const float o0 = rbf(gg * a0) + rbf(og * rbf(s[0])), o1 = rbf(gg * a1) + rbf(og * rbf(s[1]));
      const float o2 = rbf(gg * a2) + rbf(og * rbf(s[2])), o3 = rbf(gg * a3) + rbf(og * rbf(s[3]));
      *(u32x2*)(out + (size_t)(64 * rb + 16 * lb + fr) * SD + 64 * ct + 4 * dq) = u32x2{pk2(o0, o1), pk2(o2, o3)};
    };
    s16x8 areg;
#pragma unroll
    for (int lb = 0; lb < 4; ++lb) {
      f32x4 acc[4];
#pragma unroll
      for (int i = 0; i < 7; ++i) {
        if (lb < 2) hf[(lb + 2) % 3][i] = ld_sc1(rsw, lane * 16, hb + ((lb + 2) * 56 + i) * 1024);
#pragma unroll
        for (int db = 0; db < 4; ++db) acc[db] = mfma(wf2[i][db], hf[lb % 3][i], i == 0 ? f32x4{0, 0, 0, 0} : acc[db]);
        SB();
      }
      STAMP(17 + 2 * lb);
      if (lb == 3) { if (w < 4) finalize(0); else finalize(1); }
#pragma unroll
      for (int db = 0; db < 4; ++db) ldsw<f32x4>(lds, lb * PART_LB + w * PART_W + fr * 272 + db * 64 + fg * 16, acc[db]);
      if (lb == 1) {
        if (w == 0) { poll_flags(ws, WS_AFLAG + ct * 256, 32, tag, lane); poll_flags(ws, WS_GFLAG + rb * 8, 1, tag, lane); }
      }
      if (lb == 2) ldsw<s16x8>(lds, L_AT + (tid >> 3) * 160 + (tid & 7) * 16, areg);
      bar();
      STAMP(18 + 2 * lb);
      if (lb == 1) {
        areg = ld_sc1(rsw, (u32)(64 * ct + (tid >> 3)) * 4096 + (u32)(64 * rb) * 2 + (tid & 7) * 16, WS_AT);
#pragma unroll
        for (int k = 0; k < 4; ++k) gl[k] = __uint_as_float(__builtin_amdgcn_raw_buffer_load_b32(rsw, (u32)(64 * rb + 16 * k + fr) * 4, WS_GATE, AUX_SC1));
      }
    }
    if (w < 4) finalize(2); else finalize(3);
    STAMP(25);
  }
  STAMP_FLUSH();
  asm volatile(".Lfused_end:");
}

extern "C" void kernel_launch(void* const* d_in, const int* in_sizes, int n_in, void* d_out, int out_size, void* d_ws,
                              size_t ws_size, hipStream_t stream) {
  (void)hipFuncSetAttribute((const void*)fused, hipFuncAttributeMaxDynamicSharedMemorySize, LDS_BYTES);
  fused<<<256, 512, LDS_BYTES, stream>>>((const u16*)d_in[0], (const u16*)d_in[1], (const u16*)d_in[2], (const u16*)d_in[3],
      (const u16*)d_in[4], (const u16*)d_in[5], (const u16*)d_in[6], (const u16*)d_in[7], (const u16*)d_in[8],
      (const u16*)d_in[10], (const u16*)d_in[11], (const u16*)d_in[12], (u16*)d_out, (char*)d_ws);
}
```

```cpp
#include <hip/hip_runtime.h>
#include <stdint.h>
#include <type_traits>

typedef unsigned short u16;
typedef unsigned int u32;
typedef unsigned long long u64;
typedef short s16x4 __attribute__((ext_vector_type(4)));
typedef short s16x8 __attribute__((ext_vector_type(8)));
typedef float f32x2 __attribute__((ext_vector_type(2)));
typedef float f32x4 __attribute__((ext_vector_type(4)));
typedef u32 u32x2 __attribute__((ext_vector_type(2)));
typedef u32 u32x4 __attribute__((ext_vector_type(4)));
typedef __bf16 bf16x2 __attribute__((ext_vector_type(2)));
typedef __attribute__((address_space(3))) char lds_char;
typedef __attribute__((address_space(1))) u64 g_u64;
typedef __amdgpu_buffer_rsrc_t rsrc_t;

#define STAGE 9
#define CPRIO 2
#define PACE 0

constexpr int SL = 2048, SD = 512, SN = 128, SF = 1792;

constexpr u32 WS_EPOCH = 0;
constexpr u32 WS_HFLAG = 256;
constexpr u32 WS_UFLAG = 4096;
constexpr u32 WS_WFLAG = 8192;
constexpr u32 WS_GFLAG = 8704;
constexpr u32 WS_AFLAG = 10240;
constexpr u32 WS_GATE = 16384;
constexpr u32 WS_TS = 32768;
constexpr u32 WS_HF = 1u << 20;
constexpr u32 WS_W2F = WS_HF + 128u * 56u * 1024u;
constexpr u32 WS_UT = WS_W2F + 32u * 56u * 1024u;
constexpr u32 WS_AT = WS_UT + 512u * 4096u;
constexpr u32 WS_USED = WS_AT + 512u * 4096u;

constexpr u32 L_TAB = 98304;
constexpr u32 TAB_ROW = 528, TAB_CH = 44 * TAB_ROW;
constexpr u32 L_UC = 98304;
constexpr u32 UC_B = 4640, UC_CH = 4 * UC_B;
constexpr u32 L_R = 144768;
constexpr u32 R_CH = 4224;
constexpr u32 L_STG = 153216;
constexpr u32 L_MISC = 161408;
constexpr u32 PART_W = 16 * 272, PART_LB = 8 * PART_W;
constexpr u32 L_AT = 139264;
constexpr u32 L_RED = 73728;
constexpr u32 LDS_BYTES = 163840;

constexpr int AUX_SC1 = 16;
constexpr u32 SPIN_CAP = 1u << 14;
constexpr float LOG2E = 1.4426950408889634f;

__device__ __forceinline__ float bf2f(u32 v) { return __uint_as_float(v << 16); }
__device__ __forceinline__ u32 pk2(float lo, float hi) { bf16x2 p = {(__bf16)lo, (__bf16)hi}; return __builtin_bit_cast(u32, p); }
__device__ __forceinline__ float rbf(float v) { return __uint_as_float(pk2(v, v) << 16); }
__device__ __forceinline__ rsrc_t mk(const void* p, u32 n) { return __builtin_amdgcn_make_buffer_rsrc((void*)p, (short)0, (int)n, 0x00020000); }
__device__ __forceinline__ void bar() { asm volatile("s_waitcnt lgkmcnt(0)\n\ts_barrier" ::: "memory"); }
#define WAITV(n) asm volatile("s_waitcnt vmcnt(" #n ")" ::: "memory")
#define SB() __builtin_amdgcn_sched_barrier(0)
__device__ __forceinline__ void glds(rsrc_t rs, u32 dst, u32 voff, u32 soff) {
  asm volatile("s_mov_b32 m0, %0\n\ts_nop 0\n\tbuffer_load_dwordx4 %1, %2, %3 offen lds" :: "s"(dst), "v"(voff), "s"(rs), "s"(soff) : "memory");
}
__device__ __forceinline__ void glds2(rsrc_t rs, u32 dbase, int dimm, u32 voff, u32 sbase, int simm) {
  u32 so;
  asm volatile("s_add_u32 m0, %1, %2\n\ts_add_u32 %0, %5, %6\n\tbuffer_load_dwordx4 %3, %4, %0 offen lds"
               : "=&s"(so) : "s"(dbase), "i"(dimm), "v"(voff), "s"(rs), "s"(sbase), "i"(simm) : "memory", "scc");
}
__device__ __forceinline__ u32x4 ald128(rsrc_t rs, u32 voff) { u32x4 v; asm volatile("buffer_load_dwordx4 %0, %1, %2, 0 offen sc1" : "=v"(v) : "v"(voff), "s"(rs) : "memory"); return v; }
__device__ __forceinline__ u32x2 ald64(rsrc_t rs, u32 voff) { u32x2 v; asm volatile("buffer_load_dwordx2 %0, %1, %2, 0 offen sc1" : "=v"(v) : "v"(voff), "s"(rs) : "memory"); return v; }
#define READY(v) asm volatile("" : "+v"(v))
template <class T> __device__ __forceinline__ T ldsr(lds_char* b, u32 off) { return *(const __attribute__((address_space(3))) T*)(b + off); }
template <class T> __device__ __forceinline__ void ldsw(lds_char* b, u32 off, T v) { *(__attribute__((address_space(3))) T*)(b + off) = v; }
__device__ __forceinline__ s16x4 ldstr(lds_char* b, u32 off) {
  return __builtin_amdgcn_ds_read_tr16_b64_v4i16((__attribute__((address_space(3))) s16x4*)(b + off));
}
__device__ __forceinline__ s16x8 cat(s16x4 a, s16x4 b) { return __builtin_shufflevector(a, b, 0, 1, 2, 3, 4, 5, 6, 7); }
__device__ __forceinline__ f32x4 mfma(s16x8 a, s16x8 b, f32x4 c) { return __builtin_amdgcn_mfma_f32_16x16x32_bf16(a, b, c, 0, 0, 0); }
__device__ __forceinline__ s16x8 ld_sc1(rsrc_t rs, u32 voff, u32 soff) {
  return __builtin_bit_cast(s16x8, __builtin_amdgcn_raw_buffer_load_b128(rs, voff, soff, AUX_SC1));
}

__device__ __forceinline__ void flag_store(char* ws, u32 off, u64 tag) {
  __hip_atomic_store((g_u64*)(ws + off), tag, __ATOMIC_RELAXED, __HIP_MEMORY_SCOPE_AGENT);
}
__device__ __forceinline__ void poll_flags(char* ws, u32 off, int n, u64 tag, int lane) {
  g_u64* p = (g_u64*)(ws + off) + (lane < n ? lane : 0);
#pragma unroll 1
  for (u32 spin = 0; spin < SPIN_CAP; ++spin) {
    u64 v = __hip_atomic_load(p, __ATOMIC_RELAXED, __HIP_MEMORY_SCOPE_AGENT);
    if (__all(v == tag)) break;
    __builtin_amdgcn_s_sleep(1);
  }
  asm volatile("" ::: "memory");
}
__device__ __forceinline__ bool last_arrival(lds_char* lds, u32 off, u32 n, int lane) {
  u32 old = 0;
  if (lane == 0) old = __hip_atomic_fetch_add((__attribute__((address_space(3))) u32*)(lds + off), 1u, __ATOMIC_RELAXED, __HIP_MEMORY_SCOPE_WORKGROUP);
  old = __builtin_amdgcn_readfirstlane(old);
  return old == n - 1;
}
__device__ __forceinline__ u64 read_tag(lds_char* lds) {
  u32x2 t = ldsr<u32x2>(lds, L_MISC);
  return ((u64)(u32)__builtin_amdgcn_readfirstlane(t.y) << 32) | (u32)__builtin_amdgcn_readfirstlane(t.x);
}

#define STAMP(i) do {} while (0)
#define BARS(i) bar()
#define STAMP_DECL
#define STAMP_FLUSH() do {} while (0)

struct cf { float re, im; };
__device__ __forceinline__ cf cmul(cf a, cf b) {
  float t1, t2; cf r;
  asm("v_mul_f32 %0, %1, %2" : "=v"(t1) : "v"(a.im), "v"(b.im));
  asm("v_mul_f32 %0, %1, %2" : "=v"(t2) : "v"(a.im), "v"(b.re));
  asm("v_fma_f32 %0, %1, %2, -%3" : "=v"(r.re) : "v"(a.re), "v"(b.re), "v"(t1));
  asm("v_fma_f32 %0, %1, %2, %3" : "=v"(r.im) : "v"(a.re), "v"(b.im), "v"(t2));
  return r;
}
__device__ __forceinline__ cf zpow(float l2, float rev, float m) {
  float e = __builtin_amdgcn_exp2f(l2 * m);
  float a = __builtin_amdgcn_fractf(rev * m);
  cf r; r.re = e * __builtin_amdgcn_cosf(a); r.im = e * __builtin_amdgcn_sinf(a);
  return r;
}

template <int SEG> __device__ __forceinline__ void kgen_job(lds_char* lds, int c, int lane) {
  const int j = lane & 15, kq = lane >> 4;
  const int rowA = SEG == 0 ? 16 + j : (SEG == 1 ? 32 : (SEG == 2 ? 16 : 28)) + 3 * (j >> 2) + ((j & 3) < 2 ? (j & 3) : 2);
  const u32 pa = L_TAB + c * TAB_CH + rowA * TAB_ROW + kq * 16, pb = L_TAB + c * TAB_CH + j * TAB_ROW + kq * 16;
  f32x4 acc = {0, 0, 0, 0};
#pragma unroll
  for (int s = 0; s < 8; ++s) acc = mfma(ldsr<s16x8>(lds, pa + s * 64), ldsr<s16x8>(lds, pb + s * 64), acc);
  const u32 rb = L_R + c * R_CH;
  const int i = j, q = kq;
  if (SEG == 0) {
    ldsw<u32x2>(lds, rb + 2 * (2044 - 16 * i - 4 * q), u32x2{pk2(acc[3], acc[2]), pk2(acc[1], acc[0])});
  } else if (SEG == 1) {
    ldsw<u32x2>(lds, rb + 2 * (2044 - 256 - 16 * i - 4 * q), u32x2{pk2(acc[2], acc[1]), pk2(acc[0], acc[0])});
  } else if (SEG == 2) {
    const int lag0 = 512 + 256 * (q >> 1) + 16 * i + 8 * (q & 1);
    ldsw<u32x4>(lds, rb + 2 * (2040 - lag0), u32x4{pk2(acc[2], acc[2]), pk2(acc[1], acc[1]), pk2(acc[1], acc[0]), pk2(acc[0], acc[0])});
  } else {
    const int lag0 = 1024 + 256 * q + 16 * i;
    const u32 x00 = pk2(acc[0], acc[0]), x11 = pk2(acc[1], acc[1]), x22 = pk2(acc[2], acc[2]), x10 = pk2(acc[1], acc[0]);
    ldsw<u32x4>(lds, rb + 2 * (2032 - lag0), u32x4{x22, x22, x11, x11});
    ldsw<u32x4>(lds, rb + 2 * (2040 - lag0), u32x4{x11, x10, x00, x00});
  }
}

constexpr int CONV_LEAD = 10;
template <int H> __device__ __forceinline__ void conv_wave(lds_char* lds, int c, int lane, f32x4 (&acc)[8]) {
  const int r = lane & 15, kq = lane >> 4;
  u32 ub = L_UC + c * UC_CH + (lane & 3) * UC_B + 2 * (192 + 64 * ((lane >> 2) & 3) + 32 + 8 * kq) - 64 * (4 * H + 3);
  u32 kb0 = L_R + c * R_CH + 2 * (2044 - 4 * r + 32 + 8 * kq) - 64 * (4 * H + 3) - 512 * 7, kb1 = kb0 + 2048;
  asm("" : "+v"(ub)); asm("" : "+v"(kb0)); asm("" : "+v"(kb1));
  s16x8 kf[4][8], uf[4][8];
#pragma unroll
  for (int i = 0; i < 8; ++i) acc[i] = f32x4{0, 0, 0, 0};
#pragma unroll
  for (int sl = 0; sl < 4; ++sl)
#pragma unroll
    for (int j = 0; j < 8; ++j)
#pragma unroll
      for (int d = 0; d <= j; ++d) {
        const int g = 36 * sl + j * (j + 1) / 2 + d;
#pragma unroll
        for (int s2 = 0; s2 < 4; ++s2)
#pragma unroll
          for (int j2 = 0; j2 < 8; ++j2) {
            const int first = 36 * s2 + j2 * (j2 + 1) / 2 - CONV_LEAD;
            if ((first < 0 ? 0 : first) == g) {
              const u32 ko = (j2 < 4 ? kb1 : kb0) + 64 * (3 - s2) + 512 * ((7 - j2) & 3);
              kf[s2][j2] = cat(ldsr<s16x4>(lds, ko), ldsr<s16x4>(lds, ko + 8));
              uf[s2][7 - j2] = ldsr<s16x8>(lds, ub + 64 * (3 - s2) + 512 * (7 - j2));
            }
          }
        acc[7 - j + d] = mfma(uf[sl][7 - j], kf[sl][d], acc[7 - j + d]);
        SB();
      }
}

__global__ __launch_bounds__(512, 2) void fused(const u16* __restrict__ x, const u16* __restrict__ Are, const u16* __restrict__ Aim,
    const u16* __restrict__ Bre, const u16* __restrict__ Bim, const u16* __restrict__ Cre, const u16* __restrict__ Cim,
    const u16* __restrict__ logdt, const u16* __restrict__ Dskip, const u16* __restrict__ w1, const u16* __restrict__ w2,
    const u16* __restrict__ gw, u16* __restrict__ out, char* __restrict__ ws) {
  extern __shared__ __attribute__((aligned(16))) char smem[];
  lds_char* lds = (lds_char*)smem;
  const int tid = threadIdx.x, lane = tid & 63;
  const int w = __builtin_amdgcn_readfirstlane(tid >> 6);
  const int b = blockIdx.x, xcd = b & 7, idx = b >> 3;
  const bool spare = idx >= 28;

  const rsrc_t rsx = mk(x, SL * SD * 2), rsw = mk(ws, WS_USED);
  const int panel = 4 * (xcd >> 1) + (spare ? 0 : idx / 7), slice = 7 * (xcd & 1) + (spare ? 0 : idx % 7);
  const int ch0 = 64 * xcd + 2 * idx;
  const int rb = 4 * xcd + (idx >> 3), ct = idx & 7;
  const int sq = 4 * xcd + (idx - 28);
  u64 tag = 0;
  s16x8 wf2[7][4];
  STAMP_DECL;
  STAMP(0);
  if (w == 7) {
    u64 pc; u32 len;
    asm volatile("s_getpc_b64 %0\n.Lfused_beg:\n\ts_mov_b32 %1, .Lfused_end-.Lfused_beg" : "=s"(pc), "=s"(len));
    const rsrc_t rsc = mk((const void*)(pc & ~63ull), len);
    glds(rsc, L_R, lane * 16, idx * 2048);
    glds(rsc, L_R + 1024, lane * 16, idx * 2048 + 1024);
  }

  if (w < 4) {
    const int g = lane >> 4, q4 = (lane >> 2) & 3, p = lane & 3;
    const u32 v128 = (u32)(lane >> 3) * 1024 + (u32)(((lane & 7) >> 1) ^ ((lane >> 4) & 3)) * 32 + (lane & 1) * 16;
#pragma unroll
    for (int i = 0; i < 2; ++i) glds(rsx, L_STG + (w * 2 + i) * 1024, v128, (u32)(64 * idx + 16 * w + 8 * i) * 1024 + xcd * 128);
    STAMP(34);
    if (spare) {
      const rsrc_t rs2 = mk(w2, SF * SD * 2);
#pragma unroll
      for (int i = 0; i < 14; ++i) glds(rs2, (w * 14 + i) * 1024, v128, (u32)(448 * (sq & 3) + (w * 14 + i) * 8) * 1024 + (sq >> 2) * 128);
    }
    float sd[6];
    {
      const int d = ch0 + (w >> 1), o = d * SN + (tid & 127);
      const float dt = rbf(__expf(bf2f(logdt[d])));
      const float ar = dt * bf2f(Are[o]), ai = dt * bf2f(Aim[o]), br = dt * bf2f(Bre[o]), bi = dt * bf2f(Bim[o]);
      const float cr = bf2f(Cre[o]), ci = bf2f(Cim[o]);
      const float l2 = ar * LOG2E, rev = ai * 0.15915494309189535f;
      cf z = zpow(l2, rev, 1.f), z256 = zpow(l2, rev, 256.f);
      sd[0] = z.re; sd[1] = z.im; sd[2] = z256.re; sd[3] = z256.im;
      sd[4] = cr * br - ci * bi; sd[5] = cr * bi + ci * br;
    }
    STAMP(35);
    WAITV(0);
    STAMP(36);
    {
#pragma unroll
      for (int m = 0; m < 2; ++m) {
        const int cb = 2 * m + (g >> 1), r0 = 8 * (g & 1) + q4, r1 = r0 + 4;
        s16x4 lo = ldstr(lds, L_STG + w * 2048 + r0 * 128 + ((cb ^ ((r0 >> 1) & 3)) * 32) + p * 8);
        s16x4 hi = ldstr(lds, L_STG + w * 2048 + r1 * 128 + ((cb ^ ((r1 >> 1) & 3)) * 32) + p * 8);
        u32 off = WS_UT + (u32)(64 * xcd + 16 * cb + (lane & 15)) * 4096 + (u32)(64 * idx + 16 * w + 8 * (g & 1)) * 2;
        __builtin_amdgcn_raw_buffer_store_b128(__builtin_bit_cast(u32x4, cat(lo, hi)), rsw, off, 0, AUX_SC1);
      }
      asm volatile("s_waitcnt lgkmcnt(0)" ::: "memory");
#pragma unroll
      for (int k = 0; k < 6; ++k) ldsw<float>(lds, L_STG + w * 2048 + k * 256 + lane * 4, sd[k]);
    }
    STAMP(37);
    __builtin_amdgcn_s_setprio(CPRIO);
    BARS(1);
    tag = read_tag(lds);
    STAMP(2);
    if (!spare) {
      const int wf = w & 1, wl = w >> 1;
      const int r = lane & 15;
      const u32 xb = wl * 8192 + r * 128 + (((g ^ (r >> 1)) & 7) << 4);
      const int swz = q4 | ((g & 1) << 2);
      u32 wb[4];
#pragma unroll
      for (int ft = 0; ft < 4; ++ft) wb[ft] = 16384 + (8 * g + q4) * 256 + (((4 * wf + ft) ^ swz) << 5) + p * 8;
      f32x4 acc[4][4];
#pragma unroll
      for (int i = 0; i < 4; ++i)
#pragma unroll
        for (int j = 0; j < 4; ++j) acc[i][j] = f32x4{0, 0, 0, 0};
      s16x8 fa[2][4], fb[2][4];
      auto rdA = [&](int j, int ft) { u32 o = ((j >> 1) % 3) * 32768 + (j & 1) * 8192; return cat(ldstr(lds, wb[ft] + o), ldstr(lds, wb[ft] + o + 1024)); };
      auto rdB = [&](int j, int lt) { u32 o = ((j >> 1) % 3) * 32768 + lt * 2048; return ldsr<s16x8>(lds, (xb ^ ((j & 1) << 6)) + o); };
#pragma unroll
      for (int i = 0; i < 4; ++i) { fa[0][i] = rdA(0, i); fb[0][i] = rdB(0, i); }
#pragma unroll
      for (int j = 0; j < 16; ++j) {
        const int c = j & 1, n = c ^ 1;
        if (j == 3) WAITV(0);
        if (c == 1 && j < 15) BARS(8 + (j >> 1));
        if (j == 3 && w == 0 && lane == 0) flag_store(ws, WS_UFLAG + (xcd * 32 + idx) * 8, tag);
        SB();
#pragma unroll
        for (int m = 0; m < 16; ++m) {
          const int ft = m >> 2, lt = m & 3;
          acc[ft][lt] = mfma(fa[c][ft], fb[c][lt], acc[ft][lt]);
          if (j < 15) {
            if (m < 4) fb[n][m] = rdB(j + 1, m);
            else if (m < 8) fa[n][m - 4] = rdA(j + 1, m - 4);
          }
          SB();
        }
      }
      BARS(3);
      STAMP(4);
#pragma unroll
      for (int m = 0; m < 2; ++m)
#pragma unroll
        for (int lt = 0; lt < 4; ++lt) {
          f32x2 v[4], t[4];
          v[0] = acc[2 * m][lt].xy; v[1] = acc[2 * m][lt].zw; v[2] = acc[2 * m + 1][lt].xy; v[3] = acc[2 * m + 1][lt].zw;
#pragma unroll
          for (int i = 0; i < 4; ++i) t[i] = v[i] * -LOG2E;
          SB();
#pragma unroll
          for (int i = 0; i < 4; ++i) { t[i].x = __builtin_amdgcn_exp2f(t[i].x); t[i].y = __builtin_amdgcn_exp2f(t[i].y); }
          SB();
#pragma unroll
          for (int i = 0; i < 4; ++i) t[i] = t[i] + 1.f;
          SB();
#pragma unroll
          for (int i = 0; i < 4; ++i) { t[i].x = __builtin_amdgcn_rcpf(t[i].x); t[i].y = __builtin_amdgcn_rcpf(t[i].y); }
          SB();
          u32x4 o;
#pragma unroll
          for (int i = 0; i < 4; ++i) { f32x2 s = v[i] * t[i]; o[i] = pk2(s.x, s.y); }
          u32 off = WS_HF + (u32)((panel * 8 + 4 * wl + lt) * 56 + slice * 4 + 2 * wf + m) * 1024;
          __builtin_amdgcn_raw_buffer_store_b128(o, rsw, lane * 16 + off, 0, AUX_SC1);
          SB();
        }
      BARS(5);
      __builtin_amdgcn_s_setprio(0);
      WAITV(0);
      STAMP(6);
      if (last_arrival(lds, L_MISC + 16, 4, lane) && lane == 0) flag_store(ws, WS_HFLAG + (panel * 16 + slice) * 8, tag);
    } else {
#pragma unroll
      for (int si = 0; si < 14; ++si) {
        if (si == 5) bar();
        if (si == 10) {
          WAITV(10); bar();
          if (w == 0 && lane == 0) flag_store(ws, WS_UFLAG + (xcd * 32 + idx) * 8, tag);
        }
        const int r0 = 32 * si + 4 * g + q4, r1 = r0 + 16;
        s16x4 lo = ldstr(lds, r0 * 128 + ((w ^ ((r0 >> 1) & 3)) * 32) + p * 8);
        s16x4 hi = ldstr(lds, r1 * 128 + ((w ^ ((r1 >> 1) & 3)) * 32) + p * 8);
        u32 off = WS_W2F + (u32)((4 * (sq >> 2) + w) * 56 + 14 * (sq & 3) + si) * 1024;
        __builtin_amdgcn_raw_buffer_store_b128(__builtin_bit_cast(u32x4, cat(lo, hi)), rsw, lane * 16 + off, 0, AUX_SC1);
      }
      bar();
      WAITV(0);
      bar();
      if (w == 0 && lane == 0) flag_store(ws, WS_WFLAG + sq * 8, tag);
#pragma unroll
      for (int i = 5; i <= 8; ++i) bar();
      __builtin_amdgcn_s_setprio(0);
      STAMP(4);
      {
        const u32x4 gv = *(const u32x4*)(gw + lane * 8);
        float keep = 0.f;
#pragma unroll
        for (int h = 0; h < 2; ++h) {
          u32x4 xr[8];
#pragma unroll
          for (int i = 0; i < 8; ++i) xr[i] = *(const u32x4*)(x + (size_t)(64 * sq + 16 * w + 8 * h + i) * SD + lane * 8);
#pragma unroll
          for (int i = 0; i < 8; ++i) {
            float s = 0.f;
#pragma unroll
            for (int k = 0; k < 4; ++k) {
              u32 pr = pk2(bf2f(xr[i][k] & 0xffffu) * bf2f(gv[k] & 0xffffu), __uint_as_float(xr[i][k] & 0xffff0000u) * __uint_as_float(gv[k] & 0xffff0000u));
              s += __uint_as_float(pr << 16) + __uint_as_float(pr & 0xffff0000u);
            }
#pragma unroll
            for (int o = 32; o >= 1; o >>= 1) s += __shfl_xor(s, o);
            s = rbf(s);
            s = rbf(1.f / (1.f + __expf(-s)));
            if (lane == 8 * h + i) keep = s;
          }
        }
        if (lane < 16) __builtin_amdgcn_raw_buffer_store_b32(__float_as_uint(keep), rsw, (u32)(64 * sq + 16 * w + lane) * 4, WS_GATE, AUX_SC1);
      }
      STAMP(5);
      bar();
      WAITV(0);
      if (last_arrival(lds, L_MISC + 16, 4, lane) && lane == 0) flag_store(ws, WS_GFLAG + sq * 8, tag);
      if (sq == 0 && w == 0) {
#pragma unroll
        for (int i = 0; i < 4; ++i) poll_flags(ws, WS_UFLAG + i * 512, 64, tag, lane);
        if (lane == 0) flag_store(ws, WS_EPOCH, tag);
      }
    }
#pragma unroll
    for (int i = 0; i < 7; ++i)
#pragma unroll
      for (int db = 0; db < 4; ++db) wf2[i][db] = ld_sc1(rsw, lane * 16, WS_W2F + (u32)((4 * ct + db) * 56 + 7 * w + i) * 1024);
    poll_flags(ws, WS_HFLAG + (rb >> 1) * 128, 14, tag, lane);
    STAMP(7);
  } else {
    const int aw = w - 4, t = tid - 256;
    const int c = aw >> 1, n = t & 127;
    const rsrc_t rs1 = mk(w1, SD * SF * 2);
    const u32 vx = (u32)(lane >> 3) * 1024 + (u32)((lane & 7) ^ (lane >> 4)) * 16;
    const u32 cw = (u32)((((lane & 15) >> 1) ^ (lane >> 4)) << 5) | ((lane & 1) << 4);
    const u32 vx0 = vx, vx1 = vx ^ 64, vw0 = (u32)(lane >> 4) * 3584 + cw, vw1 = (u32)(lane >> 4) * 3584 + (cw ^ 128);
    const u32 sA = (u32)aw * 4096, sX = (u32)panel * 131072 + (u32)aw * 32768, sW = (u32)aw * (16 * 3584) + (u32)slice * 256;
    float dsk;
    auto run = [&](auto SPARE) {
      constexpr bool sp = decltype(SPARE)::value;
      auto ld = [&](int s, int k) {
        if (sp) return;
        const int i = k >> 1, d = (s % 3) * 32768;
        if (k & 1) glds2(rs1, sA, d + 16384 + i * 1024, (i & 2) ? vw1 : vw0, sW, (64 * s + 4 * i) * 3584);
        else glds2(rsx, sA, d + i * 1024, (i & 1) ? vx1 : vx0, sX, i * 8192 + s * 128);
      };
      u32x2 epv = ald64(rsw, WS_EPOCH);
      STAMP(34);
      const u32 dskb = Dskip[ch0 + c];
#pragma unroll
      for (int k = 0; k < 16; ++k) ld(k >> 3, k & 7);
      STAMP(35);
      if (sp) WAITV(0); else WAITV(16);
      STAMP(36);
      READY(epv);
      dsk = bf2f(dskb);
      if (aw == 0 && lane == 0) {
        const u64 ep = (((u64)epv.y << 32) | epv.x) + 1;
        ldsw<u32x2>(lds, L_MISC, u32x2{(u32)ep, (u32)(ep >> 32)});
        ldsw<u32x4>(lds, L_MISC + 16, u32x4{0, 0, 0, 0});
        ldsw<u32x4>(lds, L_MISC + 32, u32x4{0, 0, 0, 0});
      }
      WAITV(8);
      STAMP(37);
      BARS(1);
      tag = read_tag(lds);
      STAMP(2);
      const u32 tb = L_TAB + c * TAB_CH + n * 4;
      auto put = [&](int row, cf v) { ldsw<u32>(lds, tb + row * TAB_ROW, pk2(v.re, v.im)); };
      cf z, z256, cc;
      z.re = ldsr<float>(lds, L_STG + aw * 2048 + 0 * 256 + lane * 4); z.im = ldsr<float>(lds, L_STG + aw * 2048 + 1 * 256 + lane * 4);
      z256.re = ldsr<float>(lds, L_STG + aw * 2048 + 2 * 256 + lane * 4); z256.im = ldsr<float>(lds, L_STG + aw * 2048 + 3 * 256 + lane * 4);
      cc.re = ldsr<float>(lds, L_STG + aw * 2048 + 4 * 256 + lane * 4); cc.im = ldsr<float>(lds, L_STG + aw * 2048 + 5 * 256 + lane * 4);
      cf ev[9];
      {
        ld(2, 0);
        cf pz = z;
        put(16, cf{1.f, 0.f}); put(17, z);
        ld(2, 1);
#pragma unroll
        for (int j = 2; j <= 16; ++j) {
          pz = cmul(pz, z);
          if (j < 16) put(16 + j, pz);
          if ((j & 1) == 0) ev[j >> 1] = pz;
          if ((j & 1) && j < 15) ld(2, (j + 1) >> 1);
        }
      }
      WAITV(8); BARS(9);
      {
        cf pp = cc;
        ld(3, 0);
        put(0, cf{pp.re, -pp.im});
#pragma unroll
        for (int i = 1; i < 16; ++i) {
          pp = cmul(pp, ev[8]); put(i, cf{pp.re, -pp.im});
          if (i & 1) { if (i < 15) ld(3, (i + 1) >> 1); }
        }
      }
      WAITV(8); BARS(10);
      cf z512, z768;
      {
        ld(4, 0);
        put(32, z256);
#pragma unroll
        for (int k = 1; k <= 8; ++k) {
          cf v = cmul(z256, ev[k]);
          if (k & 1) put(32 + 3 * (k >> 1) + 1, v);
          else { put(32 + 3 * ((k >> 1) - 1) + 2, v); if (k < 8) put(32 + 3 * (k >> 1), v); }
          if (k < 8) ld(4, k);
        }
        z512 = cmul(z256, z256); z768 = cmul(z512, z256);
      }
      WAITV(8); BARS(11);
      u32 q2[10];
      {
        ld(5, 0);
        if (aw & 1) kgen_job<1>(lds, c, lane); else kgen_job<0>(lds, c, lane);
#pragma unroll
        for (int a = 0; a < 2; ++a) {
          cf base = a ? z768 : z512;
          q2[5 * a] = pk2(base.re, base.im);
#pragma unroll
          for (int k = 1; k <= 4; ++k) {
            cf v = cmul(base, ev[2 * k]); q2[5 * a + k] = pk2(v.re, v.im);
            SB();
            if (4 * a + k < 8) ld(5, 4 * a + k);
          }
        }
      }
      WAITV(8); BARS(12);
      {
        ld(6, 0);
#pragma unroll
        for (int a = 0; a < 2; ++a) {
          const u32 o = tb + (16 + 6 * a) * TAB_ROW;
          ldsw<u32>(lds, o, q2[5 * a]); ldsw<u32>(lds, o + TAB_ROW, q2[5 * a + 1]); ldsw<u32>(lds, o + 2 * TAB_ROW, q2[5 * a + 2]);
          ldsw<u32>(lds, o + 3 * TAB_ROW, q2[5 * a + 2]); ldsw<u32>(lds, o + 4 * TAB_ROW, q2[5 * a + 3]); ldsw<u32>(lds, o + 5 * TAB_ROW, q2[5 * a + 4]);
          ld(6, 1 + a);
        }
        cf base = cmul(z512, z512);
#pragma unroll
        for (int q = 0; q < 4; ++q) {
          cf v8 = cmul(base, ev[4]), v16 = cmul(base, ev[8]);
          put(28 + 3 * q, base); put(28 + 3 * q + 1, v8); put(28 + 3 * q + 2, v16);
          if (q < 3) base = cmul(base, z256);
          ld(6, 3 + q);
        }
        ld(6, 7);
        if (t < 64) ldsw<u32>(lds, L_R + (t >> 5) * R_CH + 4096 + (t & 31) * 4, 0u);
      }
      WAITV(8); BARS(13);
      u32x2 pu = ald64(rsw, WS_UFLAG + (xcd * 32 + (lane & 31)) * 8);
      u32x2 pw = ald64(rsw, WS_WFLAG + (ct * 4 + (lane & 3)) * 8);
#pragma unroll
      for (int k = 0; k < 4; ++k) ld(7, k);
      if (aw & 1) kgen_job<3>(lds, c, lane); else kgen_job<2>(lds, c, lane);
#pragma unroll
      for (int k = 4; k < 8; ++k) ld(7, k);
      if (sp) WAITV(0); else WAITV(8);
      READY(pu); READY(pw);
      BARS(14);
      if (!__all(pu.x == (u32)tag && pu.y == (u32)(tag >> 32))) poll_flags(ws, WS_UFLAG + xcd * 256, 32, tag, lane);
      u32x4 uc[2]; u32x2 up[2];
#pragma unroll
      for (int k = 0; k < 2; ++k) {
        const u32 o = WS_UT + (u32)(ch0 + c) * 4096 + (u32)(n + 128 * k) * 16;
        uc[k] = ald128(rsw, o);
        up[k] = ald64(rsw, o - 8);
      }
      WAITV(4);
      BARS(15);
      if (t < 192) ldsw<u32x4>(lds, L_UC + (t / 24) * UC_B + (t % 24) * 16, u32x4{0, 0, 0, 0});
      WAITV(0);
      READY(uc[0]); READY(uc[1]); READY(up[0]); READY(up[1]);
#pragma unroll
      for (int k = 0; k < 2; ++k) {
        const int m = n + 128 * k;
        u32 p0 = up[k].x, p1 = up[k].y;
        if (m == 0) { p0 = 0; p1 = 0; }
        const u32 c0 = uc[k].x, c1 = uc[k].y, c2 = uc[k].z, c3 = uc[k].w;
        const u32 s0 = __builtin_amdgcn_alignbit(p1, p0, 16), s1 = __builtin_amdgcn_alignbit(c0, p1, 16), s2 = __builtin_amdgcn_alignbit(c1, c0, 16);
        const u32 s3 = __builtin_amdgcn_alignbit(c2, c1, 16), s4 = __builtin_amdgcn_alignbit(c3, c2, 16);
        const u32 ob = L_UC + c * UC_CH + 384 + m * 16;
        ldsw<u32x4>(lds, ob, u32x4{s0, s1, s2, s3});
        ldsw<u32x4>(lds, ob + UC_B, u32x4{p1, c0, c1, c2});
        ldsw<u32x4>(lds, ob + 2 * UC_B, u32x4{s1, s2, s3, s4});
        ldsw<u32x4>(lds, ob + 3 * UC_B, uc[k]);
      }
      if (!__all(pw.x == (u32)tag && pw.y == (u32)(tag >> 32))) poll_flags(ws, WS_WFLAG + ct * 32, 4, tag, lane);
      BARS(3);
    };
    if (spare) run(std::true_type{}); else run(std::false_type{});
    STAMP(4);
#pragma unroll
    for (int i = 0; i < 7; ++i)
#pragma unroll
      for (int db = 0; db < 4; ++db) {
        wf2[i][db] = ld_sc1(rsw, lane * 16, WS_W2F + (u32)((4 * ct + db) * 56 + 7 * w + i) * 1024);
      }
    bar();
    STAMP(5);
    auto conv = [&](auto HH) {
      constexpr int h = decltype(HH)::value;
      f32x4 cacc[8];
      conv_wave<h>(lds, c, lane, cacc);
      STAMP(6);
#pragma unroll
      for (int i = 0; i < 4; ++i) ldsw<f32x4>(lds, L_RED + aw * 4096 + i * 1024 + lane * 16, cacc[4 * (1 - h) + i]);
      asm volatile("s_waitcnt lgkmcnt(0)" ::: "memory");
      if (lane == 0) ldsw<u32>(lds, L_MISC + 32 + aw * 4, 1u);
#pragma unroll 1
      for (u32 spin = 0; spin < SPIN_CAP; ++spin) {
        u32 v = __hip_atomic_load((__attribute__((address_space(3))) u32*)(lds + L_MISC + 32 + (aw ^ 1) * 4), __ATOMIC_RELAXED, __HIP_MEMORY_SCOPE_WORKGROUP);
        if (__builtin_amdgcn_readfirstlane(v)) break;
      }
      asm volatile("" ::: "memory");
      const int r = lane & 15, q = lane >> 4;
#pragma unroll
      for (int i = 0; i < 4; ++i) {
        const f32x4 y = cacc[4 * h + i] + ldsr<f32x4>(lds, L_RED + (aw ^ 1) * 4096 + i * 1024 + lane * 16);
        const int l = 256 * (4 * h + i) + 64 * q + 4 * r;
        const u32x2 uu = ldsr<u32x2>(lds, L_UC + c * UC_CH + 3 * UC_B + 384 + l * 2);
        const float a0 = rbf(y[0]) + rbf(bf2f(uu.x & 0xffffu) * dsk), a1 = rbf(y[1]) + rbf(__uint_as_float(uu.x & 0xffff0000u) * dsk);
        const float a2 = rbf(y[2]) + rbf(bf2f(uu.y & 0xffffu) * dsk), a3 = rbf(y[3]) + rbf(__uint_as_float(uu.y & 0xffff0000u) * dsk);
        __builtin_amdgcn_raw_buffer_store_b64(u32x2{pk2(a0, a1), pk2(a2, a3)}, rsw, (u32)l * 2, WS_AT + (u32)(ch0 + c) * 4096, AUX_SC1);
      }
    };
    if (aw & 1) conv(std::integral_constant<int, 1>{}); else conv(std::integral_constant<int, 0>{});
    WAITV(0);
    if (last_arrival(lds, L_MISC + 20, 4, lane) && lane == 0) flag_store(ws, WS_AFLAG + (32 * xcd + idx) * 8, tag);
    STAMP(7);
    poll_flags(ws, WS_HFLAG + (rb >> 1) * 128, 14, tag, lane);
  }
  {
    STAMP(16);
    s16x8 hf[3][7];
    const u32 hb = WS_HF + (u32)(4 * rb * 56 + 7 * w) * 1024;
#pragma unroll
    for (int k = 0; k < 14; ++k) hf[k / 7][k % 7] = ld_sc1(rsw, lane * 16, hb + ((k / 7) * 56 + k % 7) * 1024);
    const int fr = lane & 15, fg = lane >> 4;
    float gl[4];
    auto finalize = [&](int lb) {
      const int dq = 4 * (w & 3) + fg;
      f32x4 s = ldsr<f32x4>(lds, lb * PART_LB + fr * 272 + dq * 16);
#pragma unroll
      for (int k = 1; k < 8; ++k) s += ldsr<f32x4>(lds, lb * PART_LB + k * PART_W + fr * 272 + dq * 16);
      const int q4 = (lane >> 2) & 3, p = lane & 3;
      const u32x2 av = __builtin_bit_cast(u32x2, ldstr(lds, L_AT + (4 * dq + q4) * 160 + (16 * lb + 4 * p) * 2));
      const float gg = gl[lb], og = rbf(1.f - gg);
      const float a0 = bf2f(av.x & 0xffffu), a1 = __uint_as_float(av.x & 0xffff0000u), a2 = bf2f(av.y & 0xffffu), a3 = __uint_as_float(av.y & 0xffff0000u);
      const float o0 = rbf(gg * a0) + rbf(og * rbf(s[0])), o1 = rbf(gg * a1) + rbf(og * rbf(s[1]));
      const float o2 = rbf(gg * a2) + rbf(og * rbf(s[2])), o3 = rbf(gg * a3) + rbf(og * rbf(s[3]));
      *(u32x2*)(out + (size_t)(64 * rb + 16 * lb + fr) * SD + 64 * ct + 4 * dq) = u32x2{pk2(o0, o1), pk2(o2, o3)};
    };
    s16x8 areg;
#pragma unroll
    for (int lb = 0; lb < 4; ++lb) {
      f32x4 acc[4];
#pragma unroll
      for (int i = 0; i < 7; ++i) {
        if (lb < 2) hf[(lb + 2) % 3][i] = ld_sc1(rsw, lane * 16, hb + ((lb + 2) * 56 + i) * 1024);
#pragma unroll
        for (int db = 0; db < 4; ++db) acc[db] = mfma(wf2[i][db], hf[lb % 3][i], i == 0 ? f32x4{0, 0, 0, 0} : acc[db]);
        SB();
      }
      STAMP(17 + 2 * lb);
      if (lb == 3) { if (w < 4) finalize(0); else finalize(1); }
#pragma unroll
      for (int db = 0; db < 4; ++db) ldsw<f32x4>(lds, lb * PART_LB + w * PART_W + fr * 272 + db * 64 + fg * 16, acc[db]);
      if (lb == 1) {
        if (w == 0) { poll_flags(ws, WS_AFLAG + ct * 256, 32, tag, lane); poll_flags(ws, WS_GFLAG + rb * 8, 1, tag, lane); }
      }
      if (lb == 2) ldsw<s16x8>(lds, L_AT + (tid >> 3) * 160 + (tid & 7) * 16, areg);
      bar();
      STAMP(18 + 2 * lb);
      if (lb == 1) {
        areg = ld_sc1(rsw, (u32)(64 * ct + (tid >> 3)) * 4096 + (u32)(64 * rb) * 2 + (tid & 7) * 16, WS_AT);
#pragma unroll
        for (int k = 0; k < 4; ++k) gl[k] = __uint_as_float(__builtin_amdgcn_raw_buffer_load_b32(rsw, (u32)(64 * rb + 16 * k + fr) * 4, WS_GATE, AUX_SC1));
      }
    }
    if (w < 4) finalize(2); else finalize(3);
    STAMP(25);
  }
  STAMP_FLUSH();
  asm volatile(".Lfused_end:");
}

extern "C" void kernel_launch(void* const* d_in, const int* in_sizes, int n_in, void* d_out, int out_size, void* d_ws,
                              size_t ws_size, hipStream_t stream) {
  (void)hipFuncSetAttribute((const void*)fused, hipFuncAttributeMaxDynamicSharedMemorySize, LDS_BYTES);
  fused<<<256, 512, LDS_BYTES, stream>>>((const u16*)d_in[0], (const u16*)d_in[1], (const u16*)d_in[2], (const u16*)d_in[3],
      (const u16*)d_in[4], (const u16*)d_in[5], (const u16*)d_in[6], (const u16*)d_in[7], (const u16*)d_in[8],
      (const u16*)d_in[10], (const u16*)d_in[11], (const u16*)d_in[12], (u16*)d_out, (char*)d_ws);
}
```

```cpp
#include <hip/hip_runtime.h>
#include <stdint.h>
#include <type_traits>

typedef unsigned short u16;
typedef unsigned int u32;
typedef unsigned long long u64;
typedef short s16x4 __attribute__((ext_vector_type(4)));
typedef short s16x8 __attribute__((ext_vector_type(8)));
typedef float f32x2 __attribute__((ext_vector_type(2)));
typedef float f32x4 __attribute__((ext_vector_type(4)));
typedef u32 u32x2 __attribute__((ext_vector_type(2)));
typedef u32 u32x4 __attribute__((ext_vector_type(4)));
typedef __bf16 bf16x2 __attribute__((ext_vector_type(2)));
typedef __attribute__((address_space(3))) char lds_char;
typedef __attribute__((address_space(1))) u64 g_u64;
typedef __amdgpu_buffer_rsrc_t rsrc_t;


constexpr int SL = 2048, SD = 512, SN = 128, SF = 1792;

constexpr u32 WS_EPOCH = 0;
constexpr u32 WS_UFLAG = 4096;
constexpr u32 WS_GFLAG = 8704;
constexpr u32 WS_AFLAG = 10240;
constexpr u32 WS_HFLAG = 16384;
constexpr u32 WS_GATE = 32768;
constexpr u32 WS_HF = 1u << 20;
constexpr u32 WS_UT = WS_HF + 128u * 56u * 1024u;
constexpr u32 WS_AT = WS_UT + 512u * 4096u;
constexpr u32 WS_USED = WS_AT + 512u * 4096u;

constexpr u32 L_TAB = 65536;
constexpr u32 TAB_P = 2016, TAB_CH = 16 * TAB_P;
constexpr u32 L_UC = 98304;
constexpr u32 UC_B = 4640, UC_CH = 4 * UC_B;
constexpr u32 L_GAP = 135424;
constexpr u32 L_R = 144768;
constexpr u32 R_CH = 4224;
constexpr u32 L_STG = 153216;
constexpr u32 L_MISC = 161408;
constexpr u32 L_GATE = L_MISC + 128;
constexpr u32 L_DUMMY = L_MISC + 512;
constexpr u32 PART_W = 4096;
constexpr int NPA = 17, NCHK = 24;
constexpr int HDA = 10, HDC = 7;
constexpr u32 L_AT = L_GAP, AT_P = 144;
constexpr u32 LDS_BYTES = 163840;

constexpr int AUX_SC1 = 16;
constexpr u32 SPIN_CAP = 1u << 14;
constexpr u32 LDS_CAP = 1u << 23;
constexpr float LOG2E = 1.4426950408889634f;

__device__ __forceinline__ float bf2f(u32 v) { return __uint_as_float(v << 16); }
__device__ __forceinline__ u32 pk2(float lo, float hi) { bf16x2 p = {(__bf16)lo, (__bf16)hi}; return __builtin_bit_cast(u32, p); }
__device__ __forceinline__ f32x2 rbf2(f32x2 v) {
  f32x2 r;
  asm("v_cvt_pk_bf16_f32 %0, 0, %1" : "=v"(r.x) : "v"(v.x));
  asm("v_cvt_pk_bf16_f32 %0, 0, %1" : "=v"(r.y) : "v"(v.y));
  return r;
}
__device__ __forceinline__ u32 pk2o(f32x2 v) { u32 r; asm("v_cvt_pk_bf16_f32 %0, %1, %2" : "=v"(r) : "v"(v.x), "v"(v.y)); return r; }
__device__ __forceinline__ f32x2 unpk(u32 v) { return f32x2{__uint_as_float(v << 16), __uint_as_float(v & 0xffff0000u)}; }
__device__ __forceinline__ float rbf(float v) { return __uint_as_float(pk2(v, v) << 16); }
__device__ __forceinline__ rsrc_t mk(const void* p, u32 n) { return __builtin_amdgcn_make_buffer_rsrc((void*)p, (short)0, (int)n, 0x00020000); }
__device__ __forceinline__ void bar() { asm volatile("s_waitcnt lgkmcnt(0)\n\ts_barrier" ::: "memory"); }
#define WAITV(n) asm volatile("s_waitcnt vmcnt(" #n ")" ::: "memory")
template <int N> __device__ __forceinline__ void waitn() { asm volatile("s_waitcnt vmcnt(%0)" :: "i"(N) : "memory"); }
#define SB() __builtin_amdgcn_sched_barrier(0)
__device__ __forceinline__ void glds(rsrc_t rs, u32 dst, u32 voff, u32 soff) {
  asm volatile("s_mov_b32 m0, %0\n\ts_nop 0\n\tbuffer_load_dwordx4 %1, %2, %3 offen lds" :: "s"(dst), "v"(voff), "s"(rs), "s"(soff) : "memory");
}
__device__ __forceinline__ void glds2(rsrc_t rs, u32 dbase, int dimm, u32 voff, u32 sbase, int simm) {
  u32 so;
  asm volatile("s_add_u32 m0, %1, %2\n\ts_add_u32 %0, %5, %6\n\tbuffer_load_dwordx4 %3, %4, %0 offen lds"
               : "=&s"(so) : "s"(dbase), "i"(dimm), "v"(voff), "s"(rs), "s"(sbase), "i"(simm) : "memory", "scc");
}
__device__ __forceinline__ void touch(const char* p) {
  asm volatile("s_mov_b32 m0, %0\n\ts_nop 0\n\tglobal_load_lds_dword %1, off" :: "s"(L_DUMMY), "v"(p) : "memory");
}
__device__ __forceinline__ u32x4 ald128(rsrc_t rs, u32 voff) { u32x4 v; asm volatile("buffer_load_dwordx4 %0, %1, %2, 0 offen sc1" : "=v"(v) : "v"(voff), "s"(rs) : "memory"); return v; }
__device__ __forceinline__ u32x2 ald64(rsrc_t rs, u32 voff) { u32x2 v; asm volatile("buffer_load_dwordx2 %0, %1, %2, 0 offen sc1" : "=v"(v) : "v"(voff), "s"(rs) : "memory"); return v; }
#define READY(v) asm volatile("" : "+v"(v))
template <class T> __device__ __forceinline__ T ldsr(lds_char* b, u32 off) { return *(const __attribute__((address_space(3))) T*)(b + off); }
template <class T> __device__ __forceinline__ void ldsw(lds_char* b, u32 off, T v) { *(__attribute__((address_space(3))) T*)(b + off) = v; }
__device__ __forceinline__ s16x4 ldstr(lds_char* b, u32 off) {
  return __builtin_amdgcn_ds_read_tr16_b64_v4i16((__attribute__((address_space(3))) s16x4*)(b + off));
}
__device__ __forceinline__ s16x8 cat(s16x4 a, s16x4 b) { return __builtin_shufflevector(a, b, 0, 1, 2, 3, 4, 5, 6, 7); }
__device__ __forceinline__ f32x4 mfma(s16x8 a, s16x8 b, f32x4 c) { return __builtin_amdgcn_mfma_f32_16x16x32_bf16(a, b, c, 0, 0, 0); }
__device__ __forceinline__ s16x8 ld_sc1(rsrc_t rs, u32 voff, u32 soff) {
  return __builtin_bit_cast(s16x8, __builtin_amdgcn_raw_buffer_load_b128(rs, voff, soff, AUX_SC1));
}

__device__ __forceinline__ u32 chrow(u32 ch, u32 o) { return ch * 4096 + ((o + (ch & 31) * 128) & 4095); }

__device__ __forceinline__ void flag_store(char* ws, u32 off, u64 tag) {
  __hip_atomic_store((g_u64*)(ws + off), tag, __ATOMIC_RELAXED, __HIP_MEMORY_SCOPE_AGENT);
}
__device__ __forceinline__ void poll_flags(char* ws, u32 off, int n, u64 tag, int lane) {
  g_u64* p = (g_u64*)(ws + off) + (lane < n ? lane : 0);
#pragma unroll 1
  for (u32 spin = 0; spin < SPIN_CAP; ++spin) {
    u64 v = __hip_atomic_load(p, __ATOMIC_RELAXED, __HIP_MEMORY_SCOPE_AGENT);
    if (__all(v == tag)) break;
    __builtin_amdgcn_s_sleep(1);
  }
  asm volatile("" ::: "memory");
}
__device__ __forceinline__ bool last_arrival(lds_char* lds, u32 off, u32 n, int lane) {
  u32 old = 0;
  if (lane == 0) old = __hip_atomic_fetch_add((__attribute__((address_space(3))) u32*)(lds + off), 1u, __ATOMIC_RELAXED, __HIP_MEMORY_SCOPE_WORKGROUP);
  old = __builtin_amdgcn_readfirstlane(old);
  return old == n - 1;
}
__device__ __forceinline__ void lds_arrive(u32 addr) {
  asm volatile("s_mov_b64 exec, 1\n\tds_add_u32 %0, %1\n\ts_mov_b64 exec, -1" :: "v"(addr), "v"(1u) : "memory");
}
template <bool SLEEP = true> __device__ __forceinline__ void lds_wait(lds_char* lds, u32 off, u32 n) {
  __attribute__((address_space(3))) u32* p = (__attribute__((address_space(3))) u32*)(lds + off);
#pragma unroll 1
  for (u32 spin = 0; spin < LDS_CAP; ++spin) {
    u32 v = __hip_atomic_load(p, __ATOMIC_RELAXED, __HIP_MEMORY_SCOPE_WORKGROUP);
    if ((u32)__builtin_amdgcn_readfirstlane(v) >= n) break;
    if (SLEEP) __builtin_amdgcn_s_sleep(1);
  }
  asm volatile("" ::: "memory");
}


typedef f32x2 cf;
#define CMUL_T(t, a, b) "v_pk_mul_f32 " t ", " a ", " b " op_sel:[1,1] op_sel_hi:[1,0] neg_lo:[0,1]\n\t"
#define CMUL_R(r, a, b, t) "v_pk_fma_f32 " r ", " a ", " b ", " t " op_sel:[0,0,0] op_sel_hi:[0,1,1]\n\t"
__device__ __forceinline__ cf cmul(cf a, cf b) {
  cf t, r;
  asm(CMUL_T("%1", "%2", "%3") CMUL_R("%0", "%2", "%3", "%1") : "=&v"(r), "=&v"(t) : "v"(a), "v"(b));
  return r;
}
__device__ __forceinline__ void cmul2(cf& r0, cf a0, cf b0, cf& r1, cf a1, cf b1) {
  cf t0, t1;
  asm(CMUL_T("%2", "%4", "%5") CMUL_T("%3", "%6", "%7") CMUL_R("%0", "%4", "%5", "%2") CMUL_R("%1", "%6", "%7", "%3")
      : "=&v"(r0), "=&v"(r1), "=&v"(t0), "=&v"(t1) : "v"(a0), "v"(b0), "v"(a1), "v"(b1));
}
__device__ __forceinline__ void cmul3(cf& r0, cf a0, cf b0, cf& r1, cf a1, cf b1, cf& r2, cf a2, cf b2) {
  cf t0, t1, t2;
  asm(CMUL_T("%3", "%6", "%7") CMUL_T("%4", "%8", "%9") CMUL_T("%5", "%10", "%11")
      CMUL_R("%0", "%6", "%7", "%3") CMUL_R("%1", "%8", "%9", "%4") CMUL_R("%2", "%10", "%11", "%5")
      : "=&v"(r0), "=&v"(r1), "=&v"(r2), "=&v"(t0), "=&v"(t1), "=&v"(t2) : "v"(a0), "v"(b0), "v"(a1), "v"(b1), "v"(a2), "v"(b2));
}
__device__ __forceinline__ cf zpow(float l2, float rev, float m) {
  float e = __builtin_amdgcn_exp2f(l2 * m);
  float a = __builtin_amdgcn_fractf(rev * m);
  return cf{e * __builtin_amdgcn_cosf(a), e * __builtin_amdgcn_sinf(a)};
}
__device__ __forceinline__ u32 ald16(const u16* base, u32 voff) {
  u32 v; asm volatile("global_load_ushort %0, %1, %2" : "=v"(v) : "v"(voff), "s"(base) : "memory"); return v;
}

constexpr int rowQ1(int k) { return 32 + (k == 0 ? 1 : k == 1 ? 0 : k == 2 ? 2 : k == 3 ? 8 : k - 1); }
constexpr int rowQ2(int a, int k) { return 41 + (a == 0 ? k : k < 2 ? 8 + k : 3 + k); }
constexpr int rowQ3(int q, int r) { return 51 + 3 * (q == 0 ? 0 : q == 3 ? 1 : q + 1) + r; }
constexpr u64 seg_rows(int seg) {
  u64 v = 0;
  for (int j = 0; j < 16; ++j) {
    const int q = j >> 2, r = (j & 3) < 2 ? (j & 3) : 2;
    const int o = seg == 0 ? j : seg == 1 ? rowQ1(2 * q + r) - 32 : seg == 2 ? rowQ2(q >> 1, 2 * (q & 1) + r) - 41 : rowQ3(q, r) - 51;
    v |= (u64)o << (4 * j);
  }
  return v;
}

constexpr int CONV_LEAD = 10;
struct ConvState { s16x8 kf[4][8], uf[4][8]; u32 ub, kb0, kb1; };
__device__ __forceinline__ void conv_init(ConvState& st, int c, int h, int lane, f32x4 (&acc)[8]) {
  const int r = lane & 15, kq = lane >> 4;
  st.ub = L_UC + c * UC_CH + (lane & 3) * UC_B + 2 * (192 + 64 * ((lane >> 2) & 3) + 32 + 8 * kq) - 64 * (4 * h + 3);
  st.kb0 = L_R + c * R_CH + 2 * (2044 - 4 * r + 32 + 8 * kq) - 64 * (4 * h + 3) - 512 * 7;
  st.kb1 = st.kb0 + 2048;
  asm("" : "+v"(st.ub)); asm("" : "+v"(st.kb0)); asm("" : "+v"(st.kb1));
#pragma unroll
  for (int i = 0; i < 8; ++i) acc[i] = f32x4{0, 0, 0, 0};
}
template <int SL, class F> __device__ __forceinline__ void conv_step(lds_char* lds, ConvState& st, f32x4 (&acc)[8], F hook) {
#pragma unroll
  for (int j = 0; j < 8; ++j)
#pragma unroll
    for (int d = 0; d <= j; ++d) {
      const int g = 36 * SL + j * (j + 1) / 2 + d;
#pragma unroll
      for (int s2 = 0; s2 < 4; ++s2)
#pragma unroll
        for (int j2 = 0; j2 < 8; ++j2) {
          const int first = 36 * s2 + j2 * (j2 + 1) / 2 - CONV_LEAD;
          if ((first < 0 ? 0 : first) == g) {
            const u32 ko = (j2 < 4 ? st.kb1 : st.kb0) + 64 * (3 - s2) + 512 * ((7 - j2) & 3);
            st.kf[s2][j2] = cat(ldsr<s16x4>(lds, ko), ldsr<s16x4>(lds, ko + 8));
            st.uf[s2][7 - j2] = ldsr<s16x8>(lds, st.ub + 64 * (3 - s2) + 512 * (7 - j2));
          }
        }
      acc[7 - j + d] = mfma(st.uf[SL][7 - j], st.kf[SL][d], acc[7 - j + d]);
      hook(j * (j + 1) / 2 + d);
      SB();
    }
}

__global__ __launch_bounds__(512, 2) void fused(const u16* __restrict__ x, const u16* __restrict__ Are, const u16* __restrict__ Aim,
    const u16* __restrict__ Bre, const u16* __restrict__ Bim, const u16* __restrict__ Cre, const u16* __restrict__ Cim,
    const u16* __restrict__ logdt, const u16* __restrict__ Dskip, const u16* __restrict__ w1, const u16* __restrict__ w2,
    const u16* __restrict__ gw, u16* __restrict__ out, char* __restrict__ ws) {
  extern __shared__ __attribute__((aligned(16))) char smem[];
  lds_char* lds = (lds_char*)smem;
  const int tid = threadIdx.x, lane = tid & 63;
  const int w = __builtin_amdgcn_readfirstlane(tid >> 6);
  const int b = blockIdx.x, xcd = b & 7, idx = b >> 3;
  if (w == 7) {
    u64 pc; u32 len;
    asm volatile("s_getpc_b64 %0\n.Lfused_beg:\n\ts_mov_b32 %1, .Lfused_end-.Lfused_beg" : "=s"(pc), "=s"(len));
    const rsrc_t rsc = mk((const void*)(pc & ~63ull), len);
    glds(rsc, L_R, lane * 16, idx * 2048);
    glds(rsc, L_R + 1024, lane * 16, idx * 2048 + 1024);
  }
  const bool spare = idx < 4;
  const int chp = idx & 31, ch0 = 64 * xcd + 2 * chp;
  const int kc = (w >> 1) & 1, kn = tid & 127;
  const u32 po = (u32)((ch0 + kc) * SN + kn) * 2;
  asm volatile("" :: "s"(x), "s"(Are), "s"(Aim), "s"(Bre), "s"(Bim), "s"(Cre), "s"(Cim), "s"(logdt), "s"(Dskip), "s"(w1), "s"(w2), "s"(gw), "s"(out), "s"(ws));
  u32 pdt = ald16(logdt, (u32)(ch0 + kc) * 2), pAre = ald16(Are, po), pAim = ald16(Aim, po);
  u32 pBre = ald16(Bre, po), pBim = ald16(Bim, po), pCre = ald16(Cre, po), pCim = ald16(Cim, po);
  int idl = idx;
  asm volatile("" : "+s"(idl));
  const int gi = spare ? 0 : idl - 4, sq = 4 * xcd + idl;
  const rsrc_t rsx = mk(x, SL * SD * 2), rsw = mk(ws, WS_USED);
  const int panel = 4 * (xcd >> 1) + gi / 7, slice = 7 * (xcd & 1) + gi % 7;
  const int cs = (3 - (int)((uintptr_t)w2 >> 7)) & 7;
  const int q2 = spare ? idl : gi / 7;
  const int rb = 4 * xcd + q2, ct = spare ? cs : (cs + 1 + gi % 7) & 7;
  u64 tag = 0;
  s16x8 wf2[7][4];
  const rsrc_t rs2 = mk(w2, SF * SD * 2);
  const u32 vW2 = (u32)(lane >> 3) * 1024 + (u32)((((lane & 7) >> 1) ^ (lane >> 4)) << 5) + (u32)(lane & 1) * 16;
  const int rot2_ = 2 * q2 + 3 * (ct >> 2), rot2 = rot2_ >= 7 ? rot2_ - 7 : rot2_;
  auto ks = [&](int i) { const int k = i + rot2; return (u32)(k >= 7 ? k - 7 : k); };
  const u32 sW2 = (u32)w * (224 * 1024) + (u32)ct * 128;
  auto stg = [&](int i, u32 slot) {
    const u32 so = __builtin_amdgcn_readfirstlane(sW2 + ks(i) * 32768), sl = __builtin_amdgcn_readfirstlane(slot);
#pragma unroll
    for (int q = 0; q < 4; ++q) glds2(rs2, sl, q * 1024, vW2, so, q * 8192);
  };
  const u32 wro = (u32)(4 * (lane >> 4) + ((lane >> 2) & 3)) * 128 + (u32)(lane & 3) * 8, wsw = (u32)(2 * (lane >> 4) + ((lane >> 3) & 1)) & 3;
  s16x4 wh[7][4][2];
  auto rdw = [&](int i, u32 slot, int n) {
    const int db = n >> 1;
    wh[i][db][n & 1] = ldstr(lds, slot + wro + ((db ^ wsw) << 5) + (n & 1) * 2048);
    if (n & 1) wf2[i][db] = cat(wh[i][db][0], wh[i][db][1]);
  };
  auto rdw8 = [&](int i, u32 slot) {
#pragma unroll
    for (int n = 0; n < 8; ++n) rdw(i, slot, n);
  };
  auto lgk0 = [&]() { asm volatile("s_waitcnt lgkmcnt(0)" ::: "memory"); };
  auto stg_roll = [&](auto NS_, auto NR_, u32 c0, auto first) {
    constexpr int NS = decltype(NS_)::value, NR = decltype(NR_)::value;
#pragma unroll
    for (int i = 0; i < NS; ++i) stg(i, c0 + i * 4096);
    first();
#pragma unroll
    for (int i = 0; i < NR; ++i) {
      const int fly = 6 - i < NS - 1 ? 6 - i : NS - 1;
      if (fly == 3) waitn<12>(); else if (fly == 2) waitn<8>(); else if (fly == 1) waitn<4>(); else waitn<0>();
      rdw8(i, c0 + (i % NS) * 4096);
      if (i + NS < 7) { lgk0(); stg(i + NS, c0 + (i % NS) * 4096); }
    }
  };
  using I2 = std::integral_constant<int, 2>; using I4 = std::integral_constant<int, 4>;
  using I5 = std::integral_constant<int, 5>; using I7 = std::integral_constant<int, 7>;

  s16x8 hf[2][7];
  auto poll_h = [&]() {
    g_u64* hp = (g_u64*)(ws + WS_HFLAG + (u32)(rb * 32) * 8) + ((7 * w) >> 1) + 14 * ((lane >> 2) & 1) + (lane & 3);
#pragma unroll 1
    for (u32 spin = 0; spin < SPIN_CAP; ++spin) {
      u64 v = __hip_atomic_load(hp, __ATOMIC_RELAXED, __HIP_MEMORY_SCOPE_AGENT);
      if (__all(v == tag)) break;
      __builtin_amdgcn_s_sleep(1);
    }
    asm volatile("" ::: "memory");
    if (lane == 0) ldsw<u32>(lds, L_MISC + 64 + w * 4, 1u);
  };
  const int rotl = ct;
  auto lbr = [&](int lb) { return (u32)((lb + rotl) & 3); };
  auto ld_h = [&](int lb, int i) { hf[lb & 1][i] = ld_sc1(rsw, lane * 16, WS_HF + ((4 * rb + lbr(lb)) * 56 + 7 * w + ks(i)) * 1024); };
  auto ld_h01 = [&](auto HD_) {
#pragma unroll
    for (int k = 0; k < decltype(HD_)::value; ++k) ld_h(k / 7, k % 7);
  };

  u32 tb0 = L_TAB + kc * TAB_CH + (kn >> 3) * TAB_P + (kn & 7) * 4, tb1 = tb0 + 1024;
  asm("" : "+v"(tb0)); asm("" : "+v"(tb1));
  auto put = [&](int row, cf v) { ldsw<u32>(lds, row < 32 ? tb0 + row * 32 : tb1 + (row - 32) * 32, pk2(v.x, v.y)); };
  auto kjob = [&](auto mid) {
    const int c = w >> 2, seg = w & 3, j = lane & 15;
    const u64 rows = seg == 0 ? seg_rows(0) : seg == 1 ? seg_rows(1) : seg == 2 ? seg_rows(2) : seg_rows(3);
    const u32 row0 = seg == 0 ? 16 : seg == 1 ? 32 : seg == 2 ? 41 : 51;
    const u32 rowA = row0 + ((u32)(rows >> (4 * j)) & 15);
    const u32 base = L_TAB + c * TAB_CH + (lane >> 5) * TAB_P + ((lane >> 4) & 1) * 16;
    u32 pa = base + rowA * 32, pb = base + j * 32;
    asm("" : "+v"(pa)); asm("" : "+v"(pb));
    s16x8 fa[8], fb[8];
#pragma unroll
    for (int s = 0; s < 8; ++s) { fa[s] = ldsr<s16x8>(lds, pa + s * 2 * TAB_P); fb[s] = ldsr<s16x8>(lds, pb + s * 2 * TAB_P); }
    const u32x2 tg = ldsr<u32x2>(lds, L_MISC);
    SB();
    mid();
    SB();
    f32x4 a0 = {0, 0, 0, 0}, a1 = {0, 0, 0, 0};
#pragma unroll
    for (int s = 0; s < 8; s += 2) { a0 = mfma(fa[s], fb[s], a0); a1 = mfma(fa[s + 1], fb[s + 1], a1); }
    const f32x4 acc = a0 + a1;
    tag = ((u64)(u32)__builtin_amdgcn_readfirstlane(tg.y) << 32) | (u32)__builtin_amdgcn_readfirstlane(tg.x);
    const u32 rbase = L_R + c * R_CH;
    const int i = j, qq = lane >> 4;
    if (seg == 0) {
      ldsw<u32x2>(lds, rbase + 2 * (2044 - 16 * i - 4 * qq), u32x2{pk2(acc[3], acc[2]), pk2(acc[1], acc[0])});
    } else if (seg == 1) {
      ldsw<u32x2>(lds, rbase + 2 * (2044 - 256 - 16 * i - 4 * qq), u32x2{pk2(acc[2], acc[1]), pk2(acc[0], acc[0])});
    } else if (seg == 2) {
      const int lag0 = 512 + 256 * (qq >> 1) + 16 * i + 8 * (qq & 1);
      ldsw<u32x4>(lds, rbase + 2 * (2040 - lag0), u32x4{pk2(acc[2], acc[2]), pk2(acc[1], acc[1]), pk2(acc[1], acc[0]), pk2(acc[0], acc[0])});
    } else {
      const int lag0 = 1024 + 256 * qq + 16 * i;
      const u32 x00 = pk2(acc[0], acc[0]), x11 = pk2(acc[1], acc[1]), x22 = pk2(acc[2], acc[2]), x10 = pk2(acc[1], acc[0]);
      ldsw<u32x4>(lds, rbase + 2 * (2032 - lag0), u32x4{x22, x22, x11, x11});
      ldsw<u32x4>(lds, rbase + 2 * (2040 - lag0), u32x4{x11, x10, x00, x00});
    }
  };

  const int aw = w & 3;
  const rsrc_t rs1 = mk(w1, SD * SF * 2);
  const u32 vx = (u32)(lane >> 3) * 1024 + (u32)((lane & 7) ^ (lane >> 4)) * 16;
  const u32 cw = (u32)((((lane & 15) >> 1) ^ (lane >> 4)) << 5) | ((lane & 1) << 4);
  const u32 vx0 = vx, vx1 = vx ^ 64, vw0 = (u32)(lane >> 4) * 3584 + cw, vw1 = (u32)(lane >> 4) * 3584 + (cw ^ 128);
  const u32 sA = (u32)aw * 4096, sX = (u32)panel * 131072 + (u32)aw * 32768, sW = (u32)aw * (16 * 3584) + (u32)slice * 256;
  const u32 rot1 = (2 * (panel & 3) + gi % 7) & 7;
  auto ldk = [&](int s, int k) {
    const int i = k >> 1, d = (s % 3) * 32768;
    const u32 kc_ = (s + rot1) & 7;
    if (k & 1) glds2(rs1, sA, d + 16384 + i * 1024, (i & 2) ? vw1 : vw0, sW + kc_ * (64 * 3584), 4 * i * 3584);
    else glds2(rsx, sA, d + i * 1024, (i & 1) ? vx1 : vx0, sX + kc_ * 128, i * 8192);
  };
  auto ld = [&](int s) {
#pragma unroll
    for (int k = 0; k < 8; ++k) ldk(s, k);
  };
  auto touch_w2 = [&](int half, int u) {
    const int n = u * 64 + lane;
    touch((const char*)w2 + (size_t)(896 * half + 224 * ((n >> 8) & 3) + 32 * (n >> 10) + ((n >> 3) & 31)) * 1024 + (n & 7) * 128);
  };
  const int g = lane >> 4, q4 = (lane >> 2) & 3, p = lane & 3;
  u32 dskb = 0;
  u32x2 epv = {0, 0};

  const int urb = 8 * (xcd >> 1) + (idl & 7), ucb = 4 * (xcd & 1) + (idl >> 3);
  cf z, z2, z256, z512;
  float dt;
  auto setup = [&]() {
    READY(pdt); READY(pAre); READY(pAim); READY(pBre); READY(pBim); READY(pCre); READY(pCim);
    dt = rbf(__expf(bf2f(pdt)));
    const float l2 = dt * bf2f(pAre) * LOG2E, rev = dt * bf2f(pAim) * 0.15915494309189535f;
    z = zpow(l2, rev, 1.f); z256 = zpow(l2, rev, 256.f);
    cmul2(z2, z, z, z512, z256, z256);
  };
  auto touch_u = [&](int u) { touch_w2(u < 112 ? 1 : 0, u < 112 ? u : u - 112); };
  auto touch_spare = [&](int n) { const int u = 32 * 3 + n * 16 + 4 * idl + w; touch_u(u < 224 ? u : 223); };

  if (w < 4) {
#pragma unroll
    for (int i = 0; i < 2; ++i) glds(rsx, L_STG + (w * 2 + i) * 1024, (u32)(lane >> 3) * 1024 + (u32)((lane & 7) ^ ((2 * w + i) & 7)) * 16, (u32)(64 * urb + 16 * w + 8 * i) * 1024 + ucb * 128);
    touch((const char*)w2 + (size_t)((b * 4 + w) * 14 + (lane < 14 ? lane : 13)) * 128);
    ldsw<u32>(lds, L_R + (lane >> 5) * R_CH + 4096 + (lane & 31) * 4, 0u);
    waitn<2 + 1>();
  } else {
    epv = ald64(rsw, WS_EPOCH);
    dskb = ald16(Dskip, (u32)(ch0 + (aw >> 1)) * 2);
    if (spare) {
      WAITV(2);
    } else {
      ld(0);
      WAITV(10);
    }
  }
  setup();
  int wt = w;
  asm volatile("" : "+s"(wt));
  if (wt >= 4) {
    {
      cf ev = z2, od = z;
      put(16, cf{1.f, 0.f}); put(17, z);
#pragma unroll
      for (int k = 1; k < 8; ++k) {
        put(16 + 2 * k, ev);
        if (k < 7) cmul2(od, od, z2, ev, ev, z2); else od = cmul(od, z2);
        put(17 + 2 * k, od);
      }
      if (spare) WAITV(0);
      else { ld(1); WAITV(16); }
      READY(epv); READY(dskb);
      if (aw == 0 && lane == 0) {
        const u64 ep = (((u64)epv.y << 32) | epv.x) + 1;
        ldsw<u32x2>(lds, L_MISC, u32x2{(u32)ep, (u32)(ep >> 32)});
        ldsw<u32x4>(lds, L_MISC + 16, u32x4{0, 0, 0, 0});
        ldsw<u32x4>(lds, L_MISC + 32, u32x4{0, 0, 0, 0});
        ldsw<u32x4>(lds, L_MISC + 48, u32x4{0, 0, 0, 0});
        ldsw<u32x4>(lds, L_MISC + 64, u32x4{0, 0, 0, 0});
        ldsw<u32x4>(lds, L_MISC + 80, u32x4{0, 0, 0, 0});
        ldsw<u32x4>(lds, L_MISC + 96, u32x4{0, 0, 0, 0});
        ldsw<u32x4>(lds, L_MISC + 112, u32x4{0, 0, 0, 0});
      }
    }
  } else {
    {
      cf cc, z4, z768, z8, z16, z32, q3, q1 = z256;
      cmul3(cc, cf{bf2f(pCre), bf2f(pCim)}, cf{dt * bf2f(pBre), dt * bf2f(pBim)}, z4, z2, z2, z768, z512, z256);
      cf qa = z512, qb = z768, p0 = {cc.x, -cc.y}, p1;
      put(rowQ2(0, 0), qa); put(rowQ2(1, 0), qb); put(0, p0); put(rowQ1(0), q1);
      cmul3(z8, z4, z4, qa, qa, z4, qb, qb, z4); put(rowQ2(0, 1), qa); put(rowQ2(1, 1), qb);
      cmul3(z16, z8, z8, qa, qa, z4, qb, qb, z4); put(rowQ2(0, 2), qa); put(rowQ2(1, 2), qb);
      cmul3(z32, z16, z16, qa, qa, z4, qb, qb, z4); put(rowQ2(0, 3), qa); put(rowQ2(1, 3), qb);
      const cf z16c = {z16.x, -z16.y}, z32c = {z32.x, -z32.y};
      cmul3(p1, p0, z16c, qa, qa, z4, qb, qb, z4); put(rowQ2(0, 4), qa); put(rowQ2(1, 4), qb); put(1, p1);
#pragma unroll
      for (int i = 2; i < 16; i += 2) {
        cmul3(p0, p0, z32c, p1, p1, z32c, q1, q1, z2); put(i, p0); put(i + 1, p1); put(rowQ1(i / 2), q1);
      }
      cmul2(q1, q1, z2, q3, z512, z512); put(rowQ1(8), q1);
#pragma unroll
      for (int q = 0; q < 4; ++q) {
        cf r8, r16, qn;
        if (q < 3) cmul3(r8, q3, z8, r16, q3, z16, qn, q3, z256); else cmul2(r8, q3, z8, r16, q3, z16);
        put(rowQ3(q, 0), q3); put(rowQ3(q, 1), r8); put(rowQ3(q, 2), r16);
        if (q < 3) q3 = qn;
      }
    }
  }
  bar();
  kjob([&]() {
    if (w < 4) {
      if (w < 3) touch_u(3 * idx + w);
      waitn<1>();
      lds_arrive(L_MISC + 52);
    }
  });
  u32x2 ulo[2], uhi[2];
  u32x4 uo[2];
  auto u_rd = [&]() {
    const int k = 4 * ((lane >> 4) & 1) + (lane & 3);
#pragma unroll
    for (int m = 0; m < 2; ++m) {
      const u32 a = L_STG + (8 * k + ((lane >> 2) & 3)) * 128 + (((2 * w + m) ^ k) * 16) + (lane >> 5) * 8;
      ulo[m] = __builtin_bit_cast(u32x2, ldstr(lds, a)); uhi[m] = __builtin_bit_cast(u32x2, ldstr(lds, a + 512));
    }
  };
  auto u_pm = [&]() {
    const int sl = ((lane & 32) + 4 * (lane & 7) + ((lane >> 3) & 3)) * 4;
#pragma unroll
    for (int m = 0; m < 2; ++m)
      uo[m] = u32x4{(u32)__builtin_amdgcn_ds_bpermute(sl, ulo[m].x), (u32)__builtin_amdgcn_ds_bpermute(sl, ulo[m].y),
                    (u32)__builtin_amdgcn_ds_bpermute(sl, uhi[m].x), (u32)__builtin_amdgcn_ds_bpermute(sl, uhi[m].y)};
  };
  auto u_st = [&]() {
#pragma unroll
    for (int m = 0; m < 2; ++m)
      __builtin_amdgcn_raw_buffer_store_b128(uo[m], rsw, WS_UT + chrow(64 * ucb + 16 * w + 8 * m + (lane >> 3), (u32)urb * 128 + (lane & 7) * 16), 0, AUX_SC1);
  };
  u32 xb[2][2], wb[2][4];
  if (w < 4) {
    const int wf = w & 1, wl = w >> 1, r = lane & 15;
    const int swz = q4 | ((g & 1) << 2);
    lds_wait<false>(lds, L_MISC + 52, 4);
    u_rd();
    SB();
#pragma unroll
    for (int hi = 0; hi < 2; ++hi) {
#pragma unroll
      for (int k = 0; k < 2; ++k) { xb[hi][k] = ((wl * 8192 + r * 128 + (((g ^ (r >> 1)) & 7) << 4)) ^ (k << 6)) + hi * 65536; asm volatile("" : "+v"(xb[hi][k])); }
#pragma unroll
      for (int ft = 0; ft < 4; ++ft) { wb[hi][ft] = 16384 + (8 * g + q4) * 256 + (((4 * wf + ft) ^ swz) << 5) + p * 8 + hi * 65536; asm volatile("" : "+v"(wb[hi][ft])); }
    }
    SB();
    u_pm();
    u_st();
    if (spare) {
      WAITV(0);
      if (last_arrival(lds, L_MISC + 16, 4, lane) && lane == 0) flag_store(ws, WS_UFLAG + (ucb * 32 + urb) * 8, tag);
    }
    __builtin_amdgcn_s_setprio(2);
  } else if (!spare) {
    WAITV(8);
  }
  bar();
  int wm = w;
  asm volatile("" : "+s"(wm));
  if (wm >= 4) {

    const int t = tid - 256;
    const int c = aw >> 1, n = t & 127;
    const u32 s0 = 65536 + (u32)aw * 8192, sx = (u32)(aw < 2 ? L_GAP : L_STG - 8192) + (u32)aw * 4096, s2 = (u32)aw * 8192, s4 = 32768 + s2;
    u32x4 uc[2]; u32x2 up[2];
    auto ld_u = [&]() {
#pragma unroll
      for (int k = 0; k < 2; ++k) {
        const u32 o = (u32)(n + 128 * k) * 16;
        uc[k] = ald128(rsw, WS_UT + chrow(ch0 + c, o));
        up[k] = ald64(rsw, WS_UT + chrow(ch0 + c, o - 8));
      }
    };
    auto copies = [&]() {
      READY(uc[0]); READY(uc[1]); READY(up[0]); READY(up[1]);
#pragma unroll
      for (int k = 0; k < 2; ++k) {
        const int m = n + 128 * k;
        u32 p0 = up[k].x, p1 = up[k].y;
        if (m == 0) { p0 = 0; p1 = 0; }
        const u32 c0 = uc[k].x, c1 = uc[k].y, c2 = uc[k].z, c3 = uc[k].w;
        const u32 s0 = __builtin_amdgcn_alignbit(p1, p0, 16), s1 = __builtin_amdgcn_alignbit(c0, p1, 16), s2 = __builtin_amdgcn_alignbit(c1, c0, 16);
        const u32 s3 = __builtin_amdgcn_alignbit(c2, c1, 16), s4 = __builtin_amdgcn_alignbit(c3, c2, 16);
        const u32 ob = L_UC + c * UC_CH + 384 + m * 16;
        ldsw<u32x4>(lds, ob, u32x4{s0, s1, s2, s3});
        ldsw<u32x4>(lds, ob + UC_B, u32x4{p1, c0, c1, c2});
        ldsw<u32x4>(lds, ob + 2 * UC_B, u32x4{s1, s2, s3, s4});
        ldsw<u32x4>(lds, ob + 3 * UC_B, uc[k]);
      }
    };
    if (t < 192) ldsw<u32x4>(lds, L_UC + (t / 24) * UC_B + (t % 24) * 16, u32x4{0, 0, 0, 0});
    const int h = aw & 1;
    const float dsk = bf2f(dskb);
    auto conv_tail = [&](auto HH, auto SPARE, f32x4 (&cacc)[8], float (&skf)[4][4]) {
      constexpr int hh = decltype(HH)::value;
      constexpr bool sp = decltype(SPARE)::value;
      const u32 red = sp ? s0 + 4096 : sx, redp = sp ? red + 8192 - (u32)(aw & 1) * 16384 : red + 4096 - (u32)(aw & 1) * 8192;
#pragma unroll
      for (int i = 0; i < 4; ++i) ldsw<f32x4>(lds, red + i * 1024 + lane * 16, cacc[4 * (1 - hh) + i]);
      asm volatile("" ::: "memory");
      if (lane == 0) ldsw<u32>(lds, L_MISC + 32 + aw * 4, 1u);
      const int r = lane & 15, q = lane >> 4;
#pragma unroll 1
      for (u32 spin = 0; spin < LDS_CAP; ++spin) {
        u32 v = __hip_atomic_load((__attribute__((address_space(3))) u32*)(lds + L_MISC + 32 + (aw ^ 1) * 4), __ATOMIC_RELAXED, __HIP_MEMORY_SCOPE_WORKGROUP);
        if (__builtin_amdgcn_readfirstlane(v)) break;
      }
      asm volatile("" ::: "memory");
      u32x2 av[4];
      f32x4 yp[4];
#pragma unroll
      for (int i = 0; i < 4; ++i) yp[i] = ldsr<f32x4>(lds, redp + i * 1024 + lane * 16);
      const u32 hpub = ldsr<u32>(lds, L_MISC + 64 + aw * 4);
      lds_arrive(L_MISC + 20);
#pragma unroll
      for (int i = 0; i < 4; ++i) {
        const f32x4 y = cacc[4 * hh + i] + yp[i];
        av[i] = u32x2{pk2o(rbf2(y.xy) + f32x2{skf[i][0], skf[i][1]}), pk2o(rbf2(y.zw) + f32x2{skf[i][2], skf[i][3]})};
      }
      const bool early = !sp && __builtin_amdgcn_readfirstlane(hpub);
      const bool odd = lane & 1;
#pragma unroll
      for (int i = 0; i < 4; i += 2) {
        const u32 gx = odd ? av[i].x : av[i + 1].x, gy = odd ? av[i].y : av[i + 1].y;
        const u32 nx = __builtin_amdgcn_update_dpp(0u, gx, 0xb1, 0xf, 0xf, true), ny = __builtin_amdgcn_update_dpp(0u, gy, 0xb1, 0xf, 0xf, true);
        const u32x4 o = odd ? u32x4{nx, ny, av[i + 1].x, av[i + 1].y} : u32x4{av[i].x, av[i].y, nx, ny};
        const int l = 256 * (4 * hh + i + (lane & 1)) + 64 * q + 4 * (r & 14);
        __builtin_amdgcn_raw_buffer_store_b128(o, rsw, WS_AT + chrow(ch0 + c, (u32)l * 2), 0, AUX_SC1);
      }
      return early;
    };
    auto run = [&](auto SPARE) {
      constexpr bool sp = decltype(SPARE)::value;
      if (!sp) {
        ld(2); WAITV(8); bar();
        ld(3); WAITV(8); bar();
        ld(4); WAITV(8); bar();
        ld(5); WAITV(8); bar();
        ld(6); WAITV(8); bar();
        ld(7); WAITV(8); bar();
        u32x2 pu = ald64(rsw, WS_UFLAG + (xcd * 32 + (lane & 31)) * 8);
        stg(0, s0); stg(1, s0 + 4096); stg(2, sx);
        waitn<13>();
        bar();
        waitn<12>();
        READY(pu);
        if (!__all(pu.x == (u32)tag && pu.y == (u32)(tag >> 32))) poll_flags(ws, WS_UFLAG + xcd * 256, 32, tag, lane);
        ld_u();
        stg(3, s2); stg(4, s2 + 4096);
        lds_wait<false>(lds, L_MISC + 84, 4);
        stg(5, s4); stg(6, s4 + 4096);
        waitn<16>();
        __builtin_amdgcn_s_setprio(0);
        copies();
      } else {
        stg_roll(I2{}, I7{}, s0, []() {});
        lds_arrive(L_MISC + 56);
        poll_flags(ws, WS_UFLAG + xcd * 256, 32, tag, lane);
        ld_u();
        WAITV(0);
        copies();
      }
      asm volatile("s_waitcnt lgkmcnt(0)" ::: "memory");
      lds_arrive(L_MISC + 24);
      if (!sp) {
        rdw8(0, s0); rdw8(1, s0 + 4096);
        if (lane == 0) ldsw<u32>(lds, L_MISC + 96 + aw * 4, 1u);
        rdw8(2, sx);
      }
      __builtin_amdgcn_s_setprio(0);
      lds_wait(lds, L_MISC + 24, 4);
      if (4 && !sp) lds_wait(lds, L_MISC + 48, 4);
      f32x4 cacc[8];
      float skf[4][4];
      {
        ConvState st;
        conv_init(st, c, h, lane, cacc);
        conv_step<0>(lds, st, cacc, [](int) {});
        if (!sp) waitn<0>();
        conv_step<1>(lds, st, cacc, [&](int n) {
          if (!sp && n < 32) rdw(3 + (n >> 3), (n < 16 ? s2 : s4) + ((n >> 3) & 1) * 4096, n & 7);
        });
        if (!sp) lds_arrive(L_MISC + 56);
        conv_step<2>(lds, st, cacc, [](int) {});
        u32x2 uu[4];
        conv_step<3>(lds, st, cacc, [&](int n) {
          if (n < 4) uu[n] = ldsr<u32x2>(lds, L_UC + c * UC_CH + 3 * UC_B + 384 + (u32)(256 * (4 * h + n) + 64 * (lane >> 4) + 4 * (lane & 15)) * 2);
          if (n >= 5 && (n & 1)) {
            const int i = (n - 5) >> 3, e = ((n - 5) >> 1) & 3;
            const u32 p = e < 2 ? uu[i].x : uu[i].y;
            float t = __uint_as_float(e & 1 ? p & 0xffff0000u : p << 16) * dsk;
            asm("v_cvt_pk_bf16_f32 %0, 0, %1" : "=v"(skf[i][e]) : "v"(t));
          }
        });
      }
#pragma unroll
      for (int i = 0; i < 8; ++i) READY(cacc[i]);
      __builtin_amdgcn_s_setprio(0);
      const bool early = h ? conv_tail(std::integral_constant<int, 1>{}, SPARE, cacc, skf) : conv_tail(std::integral_constant<int, 0>{}, SPARE, cacc, skf);
      if (early) {
        ld_h01(std::integral_constant<int, HDA>{});
        waitn<HDA>();
        if (last_arrival(lds, L_MISC + 80, 4, lane) && lane == 0) flag_store(ws, WS_AFLAG + (32 * xcd + chp) * 8, tag);
      } else {
        WAITV(0);
        if (last_arrival(lds, L_MISC + 80, 4, lane) && lane == 0) flag_store(ws, WS_AFLAG + (32 * xcd + chp) * 8, tag);
        if (sp && sq == 0 && aw == 0) {
          g_u64* up = (g_u64*)(ws + WS_UFLAG) + lane;
#pragma unroll 1
          for (u32 spin = 0; spin < SPIN_CAP; ++spin) {
            u64 v[4];
#pragma unroll
            for (int i = 0; i < 4; ++i) v[i] = __hip_atomic_load(up + 64 * i, __ATOMIC_RELAXED, __HIP_MEMORY_SCOPE_AGENT);
            if (__all(v[0] == tag && v[1] == tag && v[2] == tag && v[3] == tag)) break;
            __builtin_amdgcn_s_sleep(1);
          }
          if (lane == 0) flag_store(ws, WS_EPOCH, tag);
        }
        lds_wait(lds, L_MISC + 64 + aw * 4, 1);
        ld_h01(std::integral_constant<int, HDA>{});
      }
    };
    if (spare) run(std::true_type{}); else run(std::false_type{});
  } else {

    if (!spare) {
      const int wf = w & 1, wl = w >> 1;
      f32x4 acc[4][4];
#pragma unroll
      for (int i = 0; i < 4; ++i)
#pragma unroll
        for (int j = 0; j < 4; ++j) acc[i][j] = f32x4{0, 0, 0, 0};
      s16x8 fa[2][4], fb[2][4];
      auto rdA = [&](int j, int ft) { const int sl = (j >> 1) % 3; u32 o = wb[sl >> 1][ft] + (sl & 1) * 32768 + (j & 1) * 8192; return cat(ldstr(lds, o), ldstr(lds, o + 1024)); };
      auto rdB = [&](int j, int lt) { const int sl = (j >> 1) % 3; return ldsr<s16x8>(lds, xb[sl >> 1][j & 1] + (sl & 1) * 32768 + lt * 2048); };
#pragma unroll
      for (int i = 0; i < 4; ++i) { fa[0][i] = rdA(0, i); fb[0][i] = rdB(0, i); }
#pragma unroll
      for (int j = 0; j < 16; ++j) {
        const int c = j & 1, n = c ^ 1;
        if (j == 1) WAITV(0);
        if (c == 1 && j < 15) bar();
        if (j == 1 && w == 0 && lane == 0) flag_store(ws, WS_UFLAG + (ucb * 32 + urb) * 8, tag);
        SB();
#pragma unroll
        for (int m = 0; m < 16; ++m) {
          const int ft = m >> 2, lt = m & 3;
          acc[ft][lt] = mfma(fa[c][ft], fb[c][lt], acc[ft][lt]);
          if (j < 15) {
            if (m < 4) fb[n][m] = rdB(j + 1, m);
            else if (m < 8) fa[n][m - 4] = rdA(j + 1, m - 4);
            if (j == 14 && m == 8) lds_arrive(L_MISC + 84);
          }
          SB();
        }
      }
#pragma unroll
      for (int lt = 0; lt < 4; ++lt) {
#pragma unroll
        for (int m = 0; m < 2; ++m) {
          f32x2 v[4], t[4];
          v[0] = acc[2 * m][lt].xy; v[1] = acc[2 * m][lt].zw; v[2] = acc[2 * m + 1][lt].xy; v[3] = acc[2 * m + 1][lt].zw;
#pragma unroll
          for (int i = 0; i < 4; ++i) t[i] = v[i] * -LOG2E;
          SB();
#pragma unroll
          for (int i = 0; i < 4; ++i) { t[i].x = __builtin_amdgcn_exp2f(t[i].x); t[i].y = __builtin_amdgcn_exp2f(t[i].y); }
          SB();
#pragma unroll
          for (int i = 0; i < 4; ++i) t[i] = t[i] + 1.f;
          SB();
#pragma unroll
          for (int i = 0; i < 4; ++i) { t[i].x = __builtin_amdgcn_rcpf(t[i].x); t[i].y = __builtin_amdgcn_rcpf(t[i].y); }
          SB();
          u32x4 o;
#pragma unroll
          for (int i = 0; i < 4; ++i) { f32x2 s = v[i] * t[i]; o[i] = pk2(s.x, s.y); }
          u32 off = WS_HF + (u32)((panel * 8 + 4 * wl + lt) * 56 + slice * 4 + 2 * wf + m) * 1024;
          __builtin_amdgcn_raw_buffer_store_b128(o, rsw, lane * 16 + off, 0, AUX_SC1);
          if (2 * lt + m == 6) lds_arrive(L_MISC + 48);
          SB();
        }
      }
      lds_wait<false>(lds, L_MISC + 96 + w * 4, 1);
      stg_roll(I2{}, I5{}, 65536 + (u32)w * 8192, [&]() {
        waitn<8>();
        if (lane == 0) flag_store(ws, WS_HFLAG + (u32)((panel * 2 + (w >> 1)) * 32 + slice * 2 + (w & 1)) * 8, tag);
        __builtin_amdgcn_s_setprio(0);
      });
      poll_h();
      WAITV(0);
      ld_h01(std::integral_constant<int, HDC>{});
      rdw8(5, 65536 + 4096 + (u32)w * 8192); rdw8(6, 65536 + (u32)w * 8192);
      lds_arrive(L_MISC + 60);
    } else {
#pragma unroll
      for (int i = 0; i < 16; ++i) glds(rsx, (w * 16 + i) * 1024, (u32)(lane ^ i) * 16, (u32)(64 * sq + 16 * w + i) * 1024);
      glds(mk(gw, SD * 2), L_GAP + w * 1024, lane * 16, 0);
#pragma unroll
      for (int n = 0; n < 1; ++n) touch_spare(n);
      waitn<1>();
      {
        const s16x8 ones = {0x3f80, 0x3f80, 0x3f80, 0x3f80, 0x3f80, 0x3f80, 0x3f80, 0x3f80};
        f32x4 ga = {0, 0, 0, 0};
        const int r = lane & 15;
#pragma unroll
        for (int s = 0; s < 16; ++s) {
          const u32x4 xr = ldsr<u32x4>(lds, (w * 16 + r) * 1024 + (((4 * s + g) ^ r) << 4));
          const u32x4 gr = ldsr<u32x4>(lds, L_GAP + w * 1024 + ((4 * s + g) << 4));
          u32x4 pr;
#pragma unroll
          for (int k = 0; k < 4; ++k)
            pr[k] = pk2(bf2f(xr[k] & 0xffffu) * bf2f(gr[k] & 0xffffu), __uint_as_float(xr[k] & 0xffff0000u) * __uint_as_float(gr[k] & 0xffff0000u));
          ga = mfma(ones, __builtin_bit_cast(s16x8, pr), ga);
        }
        float sg = rbf(ga[0]);
        sg = rbf(1.f / (1.f + __expf(-sg)));
        if (lane < 16) __builtin_amdgcn_raw_buffer_store_b32(__float_as_uint(sg), rsw, (u32)(64 * sq + 16 * w + lane) * 4, WS_GATE, AUX_SC1);
      }
      WAITV(0);
      if (last_arrival(lds, L_MISC + 28, 4, lane) && lane == 0) flag_store(ws, WS_GFLAG + sq * 8, tag);
      lds_wait(lds, L_MISC + 28, 4);
      stg_roll(I4{}, I7{}, (u32)w * 16384, [&]() {
#pragma unroll 1
        for (int n = 1; n < 2; ++n) { touch_spare(n); waitn<1>(); }
        WAITV(0);
        __builtin_amdgcn_s_setprio(0);
      });
      lds_arrive(L_MISC + 60);
      poll_h();
      ld_h01(std::integral_constant<int, HDC>{});
    }
#pragma unroll 1
    for (u32 spin = 0; spin < LDS_CAP; ++spin) {
      const u64 v = __hip_atomic_load((__attribute__((address_space(3))) u64*)(lds + L_MISC + 56), __ATOMIC_RELAXED, __HIP_MEMORY_SCOPE_WORKGROUP);
      if (__builtin_amdgcn_readfirstlane((u32)v) >= 4 && __builtin_amdgcn_readfirstlane((u32)(v >> 32)) >= 4) break;
    }
    asm volatile("" ::: "memory");
  }
  {
    const u32 fo = lane < 8 ? WS_AFLAG + (u32)(32 * ct + 8 * w + lane) * 8 : WS_GFLAG + (u32)rb * 8;
    u32x2 pa;
    const int fr = lane & 15, fg = lane >> 4;
    u32 pw[4];
#pragma unroll
    for (int k = 0; k < 4; ++k) {
      pw[k] = (u32)(w & 3) * PART_W + fr * 256 + (((4 * k + fg) ^ fr) << 4);
      asm("" : "+v"(pw[k]));
    }
    f32x4 acc[2][4];
    u32x4 areg[2], greg[2];
    const int fl = w >> 1;
    auto g2 = [&](auto COMPUTE) {
      constexpr bool cw = decltype(COMPUTE)::value;
      constexpr int HD = cw ? HDC : HDA;
      f32x4 pc[4];
      u32 pf = 0;
      const u32 pflag = L_MISC + 96 + (u32)(w & 3) * 4, pbase = spare ? 1 : 2;
      auto part_w = [&](int lb, int k) {
        f32x4 v = acc[lb & 1][k];
        if (!cw) {
#pragma unroll
          for (int e = 0; e < 4; ++e) { float t = v[e] + pc[k][e]; asm("" : "+v"(t)); v[e] = t; }
        }
        ldsw<f32x4>(lds, pw[k] + lb * 4 * PART_W, v);
      };
      u32 ao[2], go = (u32)(64 * rb + 8 * (lane & 7)) * 4;
#pragma unroll
      for (int m = 0; m < 2; ++m) {
        ao[m] = chrow(64 * ct + 16 * w + 8 * m + (lane >> 3), (u32)(64 * rb) * 2 + (lane & 7) * 16);
        if (cw) asm volatile("" : "+v"(ao[m]));
      }
      lds_char* atw = lds + L_AT + (16 * w + (lane >> 3)) * AT_P + (lane & 7) * 16;
      if (cw) asm volatile("" : "+v"(go), "+v"(atw));
      u32 hso[7], hvo = 0;
#pragma unroll
      for (int i = 0; i < 7; ++i) { hso[i] = __builtin_amdgcn_readfirstlane(WS_HF + (4 * rb * 56 + 7 * w + ks(i)) * 1024); asm volatile("" : "+s"(hso[i])); }
      auto a_tile = [&]() {
        waitn<(NCHK < 28 - HD ? NCHK : 28 - HD) - NPA>();
        READY(pa);
        const u64 need = 0x1ff;
        if ((__ballot(pa.x == (u32)tag && pa.y == (u32)(tag >> 32)) & need) != need) {
          g_u64* fp = (g_u64*)(ws + fo);
#pragma unroll 1
          for (u32 spin = 0; spin < SPIN_CAP; ++spin) {
            u64 v = __hip_atomic_load(fp, __ATOMIC_RELAXED, __HIP_MEMORY_SCOPE_AGENT);
            if ((__ballot(v == tag) & need) == need) break;
            __builtin_amdgcn_s_sleep(1);
          }
          asm volatile("" ::: "memory");
        }
#pragma unroll
        for (int m = 0; m < 2; ++m) {
          areg[m] = __builtin_bit_cast(u32x4, ld_sc1(rsw, ao[m], WS_AT));
          greg[m] = __builtin_bit_cast(u32x4, ld_sc1(rsw, go + m * 16, WS_GATE));
        }
      };
#pragma unroll
      for (int lb = 0; lb < 4; ++lb) {
#pragma unroll
        for (int i = 0; i < 7; ++i) {
          const bool pw_ = lb >= 1 && i < 4, padd = !cw;
          f32x4 v;
          auto mm = [&](int db) { acc[lb & 1][db] = mfma(wf2[i][db], hf[lb & 1][i], i == 0 ? f32x4{0, 0, 0, 0} : acc[lb & 1][db]); SB(); };
          auto add = [&](int e) { float t = acc[(lb - 1) & 1][i][e]; if (padd) { t += pc[i][e]; asm("" : "+v"(t)); } v[e] = t; };
          auto pcr = [&](int k) { pc[k] = ldsr<f32x4>(lds, pw[k] + lb * 4 * PART_W); };
          if (cw && 7 * lb + i == NPA) pa = ald64(rsw, fo);
          if (cw && 7 * lb + i == NCHK) a_tile();
          mm(0);
          const int nl = 7 * lb + i + HD, ll = nl / 7, il = nl % 7;
          if ((il == 0 || (lb == 0 && i == 0)) && nl < 28) hvo = lane * 16 + lbr(ll) * (56 * 1024);
          if (pw_) { add(0); add(1); }
          if (padd && i == 4) {
            if ((u32)__builtin_amdgcn_readfirstlane(pf) < pbase + lb) lds_wait<false>(lds, pflag, pbase + lb);
            asm volatile("" ::: "memory");
            pcr(0);
          }
          if (padd && i == 5) pcr(2);
          SB();
          mm(1);
          if (pw_) { add(2); add(3); }
          if (padd && i == 4) pcr(1);
          if (padd && i == 5) pcr(3);
          SB();
          mm(2);
          if (pw_) ldsw<f32x4>(lds, pw[i] + (lb - 1) * 4 * PART_W, v);
          if (cw && lb >= 1 && i == 4) lds_arrive(pflag);
          SB();
          mm(3);
          if (nl < 28) hf[ll & 1][il] = ld_sc1(rsw, hvo, hso[il]);
          if (padd && i == 1) pf = __hip_atomic_load((__attribute__((address_space(3))) u32*)(lds + pflag), __ATOMIC_RELAXED, __HIP_MEMORY_SCOPE_WORKGROUP);
          SB();
        }
        if (cw && lb == 3) {
#pragma unroll
          for (int k = 0; k < 4; ++k) part_w(3, k);
          lds_arrive(pflag);
        }
      }
#pragma unroll
      for (int k = 0; k < 4; ++k) if (!cw) part_w(3, k);
      if (cw) {
        lds_wait<false>(lds, L_MISC + 20, 4);
        const f32x4 g0 = __builtin_bit_cast(f32x4, greg[0]), g1 = __builtin_bit_cast(f32x4, greg[1]);
#pragma unroll
        for (int m = 0; m < 2; ++m)
          ldsw<u32x4>(atw, 8 * m * AT_P,
                      u32x4{pk2o(unpk(areg[m].x) * g0.xy), pk2o(unpk(areg[m].y) * g0.zw), pk2o(unpk(areg[m].z) * g1.xy), pk2o(unpk(areg[m].w) * g1.zw)});
        if (w == 0 && lane < 8) {
          const f32x2 o0 = rbf2(1.f - g0.xy), o1 = rbf2(1.f - g0.zw), o2 = rbf2(1.f - g1.xy), o3 = rbf2(1.f - g1.zw);
          ldsw<f32x4>(lds, L_GATE + lane * 32, f32x4{o0.x, o0.y, o1.x, o1.y}); ldsw<f32x4>(lds, L_GATE + lane * 32 + 16, f32x4{o2.x, o2.y, o3.x, o3.y});
        }
      }
    };
    const u32 row = 16 * lbr(fl) + fr, c0 = 8 * (w & 1) + 2 * fg;
    const u32 b0 = (u32)fl * 4 * PART_W + fr * 256 + ((c0 ^ fr) << 4), b1 = b0 ^ 16;
    const u32 ba = L_AT + (4 * c0 + q4) * AT_P + (16 * lbr(fl) + 4 * p) * 2, bg = L_GATE + row * 4;
    u32 oo = ((64 * rb + row) * SD + 64 * ct + 4 * c0) * 2;
    lds_char *p0 = lds + b0, *p1 = lds + b1, *pa_ = lds + ba, *pg = lds + bg;
    rsrc_t rso = mk(out, SL * SD * 2);
    asm volatile("" : "+v"(p0), "+v"(p1), "+v"(pa_), "+v"(pg), "+v"(oo), "+s"(rso));
    if (w < 4) g2(std::true_type{}); else g2(std::false_type{});
    bar();
    {
      const float og = ldsr<float>(pg, 0);
      f32x4 s[2][4];
      u32x2 av[2];
#pragma unroll
      for (int j = 0; j < 2; ++j) {
#pragma unroll
        for (int k = 0; k < 4; ++k) s[j][k] = ldsr<f32x4>(j ? p1 : p0, k * PART_W);
        av[j] = __builtin_bit_cast(u32x2, ldstr(pa_, j * 4 * AT_P));
        SB();
      }
      u32x2 o[2];
#pragma unroll
      for (int j = 0; j < 2; ++j) {
        const f32x4 sum = (s[j][0] + s[j][1]) + (s[j][2] + s[j][3]);
        const f32x2 t01 = rbf2(rbf2(sum.xy) * og), t23 = rbf2(rbf2(sum.zw) * og);
        o[j] = u32x2{pk2o(unpk(av[j].x) + t01), pk2o(unpk(av[j].y) + t23)};
        SB();
      }
      __builtin_amdgcn_raw_buffer_store_b128(u32x4{o[0].x, o[0].y, o[1].x, o[1].y}, rso, oo, 0, 0);
    }
  }
  asm volatile(".Lfused_end:");
}

extern "C" void kernel_launch(void* const* d_in, const int* in_sizes, int n_in, void* d_out, int out_size, void* d_ws,
                              size_t ws_size, hipStream_t stream) {
  (void)hipFuncSetAttribute((const void*)fused, hipFuncAttributeMaxDynamicSharedMemorySize, LDS_BYTES);
  fused<<<256, 512, LDS_BYTES, stream>>>((const u16*)d_in[0], (const u16*)d_in[1], (const u16*)d_in[2], (const u16*)d_in[3],
      (const u16*)d_in[4], (const u16*)d_in[5], (const u16*)d_in[6], (const u16*)d_in[7], (const u16*)d_in[8],
      (const u16*)d_in[10], (const u16*)d_in[11], (const u16*)d_in[12], (u16*)d_out, (char*)d_ws);
}
```
